```python
import torch

def _softmax(x, axis=-1):
    return torch.softmax(x, dim=axis)
def _gelu(x):
    return torch.nn.functional.gelu(x, approximate="tanh")
def _silu(x):
    return torch.nn.functional.silu(x)
def _sigmoid(x):
    return torch.sigmoid(x)
def _layernorm(x, axis=-1, eps=1e-5):
    m = x.mean(dim=axis, keepdim=True)
    v = x.var(dim=axis, keepdim=True, unbiased=False)
    return (x - m) / torch.sqrt(v + eps)
def _scatter_add(tgt, idx, src, axis=0):
    return tgt.clone().index_add_(axis, idx, src)
def _segment_sum(src, seg, n):
    out = torch.zeros((n,) + src.shape[1:], dtype=src.dtype)
    return out.index_add_(0, seg, src)
def _pad(x, pw):
    flat = tuple(v for pair in reversed(pw) for v in pair)
    return torch.nn.functional.pad(x, flat)

kernel_name = 'gated_mix__ssm_conv_h3__mixer_s_mlp__prompt'

def setup_inputs():

    _w = (lambda a: (lambda t: t.to(torch.bfloat16) if t.is_floating_point() else t)(torch.as_tensor(a)))
    def _A_setup():
        B, S, D = 1, 2048, 576
        ks = 4
        return {
            "q": _w((lambda *s: torch.randn(*s, dtype=torch.bfloat16) * 0.05)(B, S, D)),
            "k": _w((lambda *s: torch.randn(*s, dtype=torch.bfloat16) * 1.6)(B, S, D)),
            "v": _w((lambda *s: torch.randn(*s, dtype=torch.bfloat16) * 1.6)(B, S, D)),
            "shift_k": _w((lambda *s: torch.randn(*s, dtype=torch.bfloat16) * 1.6)(D, ks) * 0.5),
            "ssm_k": _w((lambda *s: torch.randn(*s, dtype=torch.bfloat16) * 3.2)(D, S) * (1.0 / S ** 0.5)),
            "ssm_D": _w((lambda *s: torch.randn(*s, dtype=torch.bfloat16) * 1.6)(D)),
        }
    _A_in = _A_setup()

    def _B_setup():
        B, S, D, F = 1, 2048, 576, 4608
        return {
            "x":  _w((lambda *s: torch.randn(*s, dtype=torch.bfloat16) * 0.05)(B, S, D)),
            "w1": _w((lambda *s: torch.randn(*s, dtype=torch.bfloat16) * 3.2)(D, D) * (D ** -0.5)),
            "w2": _w((lambda *s: torch.randn(*s, dtype=torch.bfloat16) * 3.2)(D, D) * (D ** -0.5)),
            "w3": _w((lambda *s: torch.randn(*s, dtype=torch.bfloat16) * 3.2)(D, F) * (D ** -0.5)),
            "w4": _w((lambda *s: torch.randn(*s, dtype=torch.bfloat16) * 1.6)(F, D) * (F ** -0.5)),
        }
    _B_in = _B_setup()

    return {
        "A_q": _A_in["q"],
        "A_k": _A_in["k"],
        "A_v": _A_in["v"],
        "A_shift_k": _A_in["shift_k"],
        "A_ssm_k": _A_in["ssm_k"],
        "A_ssm_D": _A_in["ssm_D"],
        "B_x": _B_in["x"],
        "B_w1": _B_in["w1"],
        "B_w2": _B_in["w2"],
        "B_w3": _B_in["w3"],
        "B_w4": _B_in["w4"],
        "gate_w": _w((lambda *s: torch.randn(*s, dtype=torch.bfloat16) * 0.05)(576) * 16.0),
    }

def reference(A_q, A_k, A_v, A_shift_k, A_ssm_k, A_ssm_D, B_x, B_w1, B_w2, B_w3, B_w4, gate_w):
    def _A_ref(q, k, v, shift_k, ssm_k, ssm_D):
        B, S, D = q.shape
        ks = shift_k.shape[-1]
        k_pad = _pad(k, ((0, 0), (ks - 1, 0), (0, 0)))
        k_shift = torch.zeros_like(k)
        for t in range(ks):
            k_shift = k_shift + k_pad[:, t:t + S, :] * shift_k[:, ks - 1 - t][None, None, :]
        kv = k_shift * v
        L = 2 * S
        kv_pad = _pad(kv, ((0, 0), (0, S), (0, 0)))
        kf_pad = _pad(ssm_k, ((0, 0), (0, S)))
        kv_f = (lambda x, axes=(-2,-1): torch.fft.rfft2(x.float(), dim=axes))(kv_pad[:, None, :, :], axes=(1, 2))
        kf_f = (lambda x, axes=(-2,-1): torch.fft.rfft2(x.float(), dim=axes))(kf_pad[:, None, :], axes=(1, 2))
        kf_f = (lambda x, *d: x.permute(*d))(kf_f, 1, 2, 0)
        y_f = kv_f * kf_f[None, :, :, :]
        y = (lambda x, s, axes=(-2,-1): torch.fft.irfft2(x, s=s, dim=axes).to(torch.bfloat16))(y_f, (1, L), axes=(1, 2))[:, 0, :S, :]
        y = y + kv * ssm_D[None, None, :]
        return q * y

    def _B_ref(x, w1, w2, w3, w4):
        B, S, D = x.shape
        h = _gelu(_layernorm(x, axis=-1) @ w1)
        c = D // 4
        g0 = _pad(h[:, :, 0*c:1*c], ((0, 0), (1, 0), (0, 0)))[:, :S, :]
        g1 = _pad(h[:, :, 1*c:2*c], ((0, 0), (0, 1), (0, 0)))[:, 1:, :]
        g2 = _pad(h[:, :, 2*c:3*c], ((0, 0), (2, 0), (0, 0)))[:, :S, :]
        g3 = _pad(h[:, :, 3*c:   ], ((0, 0), (0, 2), (0, 0)))[:, 2:, :]
        hs = torch.concatenate([g0, g1, g2, g3], axis=-1) @ w2
        x1 = x + hs
        h2 = _gelu(_layernorm(x1, axis=-1) @ w3) @ w4
        return x1 + h2

    x = A_q
    g = _sigmoid((x * gate_w).sum(-1, keepdims=True))
    a = _A_ref(x, A_k, A_v, A_shift_k, A_ssm_k, A_ssm_D)
    b = _B_ref(x, B_w1, B_w2, B_w3, B_w4)
    return g * a + (1 - g) * b
```

```cpp
#include <hip/hip_runtime.h>
#include <hip/hip_bf16.h>
#include <stdint.h>

typedef unsigned short u16;
constexpr int S = 2048, D = 576, F = 4608;

__device__ __forceinline__ float bf2f(u16 v) { return __uint_as_float(((unsigned)v) << 16); }
__device__ __forceinline__ u16 f2bf(float f) {
    __hip_bfloat16 b = __float2bfloat16(f);
    return *reinterpret_cast<u16*>(&b);
}
__device__ __forceinline__ float rbf(float f) { return bf2f(f2bf(f)); }
__device__ __forceinline__ float gelu(float x) {
    float z = 0.7978845608028654f * (x + 0.044715f * x * x * x);
    return 0.5f * x * (1.f + tanhf(z));
}

__global__ void k_kv(const u16* k, const u16* v, const u16* sk, u16* kv) {
    int i = blockIdx.x * 256 + threadIdx.x;
    int s = i / D, d = i % D;
    float acc = 0.f;
    for (int t = 0; t < 4; ++t) {
        int ss = s + t - 3;
        float kk = ss >= 0 ? bf2f(k[ss * D + d]) : 0.f;
        acc = rbf(acc + rbf(kk * bf2f(sk[d * 4 + 3 - t])));
    }
    kv[i] = f2bf(acc * bf2f(v[i]));
}
__global__ void k_conv(const u16* kv, const u16* sk, const u16* sD, const u16* q, u16* a) {
    int i = blockIdx.x * 256 + threadIdx.x;
    int s = i / D, d = i % D;
    float acc = 0.f;
    for (int j = 0; j <= s; ++j) acc += bf2f(kv[(s - j) * D + d]) * bf2f(sk[d * S + j]);
    float y = rbf(rbf(acc) + rbf(bf2f(kv[i]) * bf2f(sD[d])));
    a[i] = f2bf(bf2f(q[i]) * y);
}
__global__ void k_ln(const u16* x, u16* o) {
    int s = blockIdx.x; int l = threadIdx.x;
    float sum = 0.f;
    for (int d = l; d < D; d += 64) sum += bf2f(x[s * D + d]);
    for (int m = 32; m; m >>= 1) sum += __shfl_xor(sum, m);
    float mean = sum / D;
    float vs = 0.f;
    for (int d = l; d < D; d += 64) { float t = bf2f(x[s * D + d]) - mean; vs += t * t; }
    for (int m = 32; m; m >>= 1) vs += __shfl_xor(vs, m);
    float rs = rsqrtf(vs / D + 1e-5f);
    for (int d = l; d < D; d += 64) o[s * D + d] = f2bf((bf2f(x[s * D + d]) - mean) * rs);
}
template <int MODE>
__global__ void k_gemm(const u16* A, const u16* W, u16* O, int N, int K, const u16* res, const u16* a, const u16* x, const u16* gw) {
    int n = blockIdx.x * 64 + threadIdx.x; int s = blockIdx.y;
    float acc = 0.f;
    for (int k = 0; k < K; ++k) {
        int ss = s;
        if (MODE == 1) { int g = k / 144; ss = s + (g == 0 ? -1 : g == 1 ? 1 : g == 2 ? -2 : 2); }
        float av = (ss >= 0 && ss < S) ? bf2f(A[ss * K + k]) : 0.f;
        acc += av * bf2f(W[k * N + n]);
    }
    if (MODE == 0) O[s * N + n] = f2bf(gelu(acc));
    if (MODE == 1) O[s * N + n] = f2bf(bf2f(res[s * N + n]) + rbf(acc));
    if (MODE == 2) {
        float gs = 0.f;
        for (int d = 0; d < D; ++d) gs += bf2f(x[s * D + d]) * bf2f(gw[d]);
        float g = 1.f / (1.f + expf(-gs));
        float b = bf2f(res[s * N + n]) + acc;
        O[s * N + n] = f2bf(g * bf2f(a[s * N + n]) + (1.f - g) * b);
    }
}

extern "C" void kernel_launch(void* const* d_in, const int* in_sizes, int n_in,
                              void* d_out, int out_size, void* d_ws, size_t ws_size,
                              hipStream_t stream) {
    const u16* q = (const u16*)d_in[0];
    const u16* k = (const u16*)d_in[1];
    const u16* v = (const u16*)d_in[2];
    const u16* shk = (const u16*)d_in[3];
    const u16* ssk = (const u16*)d_in[4];
    const u16* ssD = (const u16*)d_in[5];
    const u16* w1 = (const u16*)d_in[7];
    const u16* w2 = (const u16*)d_in[8];
    const u16* w3 = (const u16*)d_in[9];
    const u16* w4 = (const u16*)d_in[10];
    const u16* gw = (const u16*)d_in[11];
    u16* ws = (u16*)d_ws;
    u16* kv = ws; u16* a = ws + S * D; u16* ln = ws + 2 * S * D; u16* h = ws + 3 * S * D; u16* x1 = ws + 4 * S * D;
    u16* G = ws + 5 * S * D;
    k_kv<<<S * D / 256, 256, 0, stream>>>(k, v, shk, kv);
    k_conv<<<S * D / 256, 256, 0, stream>>>(kv, ssk, ssD, q, a);
    k_ln<<<S, 64, 0, stream>>>(q, ln);
    k_gemm<0><<<dim3(D / 64, S), 64, 0, stream>>>(ln, w1, h, D, D, nullptr, nullptr, nullptr, nullptr);
    k_gemm<1><<<dim3(D / 64, S), 64, 0, stream>>>(h, w2, x1, D, D, q, nullptr, nullptr, nullptr);
    k_ln<<<S, 64, 0, stream>>>(x1, ln);
    k_gemm<0><<<dim3(F / 64, S), 64, 0, stream>>>(ln, w3, G, F, D, nullptr, nullptr, nullptr, nullptr);
    k_gemm<2><<<dim3(D / 64, S), 64, 0, stream>>>(G, w4, (u16*)d_out, D, F, x1, a, q, gw);
}
```

```cpp
#include <hip/hip_runtime.h>
#include <hip/hip_bf16.h>
#include <stdint.h>
#include <type_traits>

typedef unsigned short u16;
typedef __attribute__((ext_vector_type(4))) short s16x4;
typedef __attribute__((ext_vector_type(8))) __bf16 bf16x8;
typedef __attribute__((ext_vector_type(2))) __bf16 bf16x2;
typedef __attribute__((ext_vector_type(4))) float f32x4;
typedef __attribute__((ext_vector_type(2))) float f32x2;
typedef __attribute__((ext_vector_type(4))) unsigned u32x4;
typedef __attribute__((ext_vector_type(2))) unsigned u32x2;
#define LDSP __attribute__((address_space(3)))
#define DI __device__ __forceinline__
#define SB() __builtin_amdgcn_sched_barrier(0)

constexpr int S = 2048, D = 576, F = 4608;
constexpr float GS = 0.46866869f;
constexpr float GALPHA = -4.91222954f;
constexpr float GINV = 2.13370343f;

template <int I, int N, class Fn> DI void sfor(Fn&& f) {
    if constexpr (I < N) { f(std::integral_constant<int, I>{}); sfor<I + 1, N>(f); }
}
DI float bf2f(u16 v) { return __uint_as_float(((unsigned)v) << 16); }
DI float bflo(unsigned v) { return __uint_as_float(v << 16); }
DI float bfhi(unsigned v) { return __uint_as_float(v & 0xffff0000u); }
DI unsigned pk2(float a, float b) { f32x2 v = {a, b}; return __builtin_bit_cast(unsigned, __builtin_convertvector(v, bf16x2)); }
DI u16 f2bf(float f) { return (u16)(pk2(f, 0.f) & 0xffffu); }
DI float rbf(float f) { return bflo(pk2(f, 0.f)); }
DI u32x4 mk_rsrc(const void* p, unsigned bytes) {
    unsigned long long a = (unsigned long long)p;
    u32x4 r = {(unsigned)a, (unsigned)(a >> 32) & 0xffffu, bytes, 0x00020000u};
    return r;
}
DI void dma16(u32x4 rs, unsigned lds, unsigned voff, unsigned soff) {
    asm volatile("s_mov_b32 m0, %0\n\ts_nop 0\n\tbuffer_load_dwordx4 %1, %2, %3 offen lds" :: "s"(lds), "v"(voff), "s"(rs), "s"(soff) : "memory");
}
template <int OFF> DI void ld16(u32x4& d, u32x4 rs, unsigned vo) {
    asm volatile("buffer_load_dwordx4 %0, %1, %2, 0 offen offset:%3" : "=v"(d) : "v"(vo), "s"(rs), "n"(OFF));
}
template <bool FIRST> DI void mfma2(f32x4& c0, f32x4& c1, const bf16x8& a, const bf16x8& b0, const bf16x8& b1) {
    if constexpr (FIRST) asm volatile("v_mfma_f32_16x16x32_bf16 %0, %2, %3, 0\n\tv_mfma_f32_16x16x32_bf16 %1, %2, %4, 0" : "=&v"(c0), "=&v"(c1) : "v"(a), "v"(b0), "v"(b1));
    else asm volatile("v_mfma_f32_16x16x32_bf16 %0, %2, %3, %0\n\tv_mfma_f32_16x16x32_bf16 %1, %2, %4, %1" : "+v"(c0), "+v"(c1) : "v"(a), "v"(b0), "v"(b1));
}
template <bool FIRST> DI void mfma1(f32x4& c0, const bf16x8& a, const bf16x8& b0) {
    if constexpr (FIRST) asm volatile("v_mfma_f32_16x16x32_bf16 %0, %1, %2, 0" : "=&v"(c0) : "v"(a), "v"(b0));
    else asm volatile("v_mfma_f32_16x16x32_bf16 %0, %1, %2, %0" : "+v"(c0) : "v"(a), "v"(b0));
}
#define STAMP(i) do {} while (0)
struct GeluT { float t0, t1, t2; };
template <int e> DI void gelu_even(GeluT& T, const f32x4* P) {
    if constexpr (e == 0) asm volatile("v_fma_f32 %0, -%1, %1, %2" : "=v"(T.t0) : "v"(P[0][0]), "s"(GALPHA));
    else if constexpr (e < 8) asm volatile("v_add_f32 %1, 1.0, %0\n\tv_fma_f32 %0, -%2, %2, %3\n\tv_rcp_f32 %1, %1" : "+v"(T.t0), "=&v"(T.t1) : "v"(P[e >> 2][e & 3]), "s"(GALPHA));
    else asm volatile("v_add_f32 %0, 1.0, %1\n\ts_nop 0\n\tv_rcp_f32 %0, %0" : "=&v"(T.t1) : "v"(T.t0));
}
template <int e> DI void gelu_odd(GeluT& T, const f32x4* P, unsigned* Go) {
    if constexpr (e == 0) asm volatile("v_mul_f32 %0, %0, %1\n\ts_nop 0\n\tv_exp_f32 %0, %0" : "+v"(T.t0) : "v"(P[0][0]));
    else if constexpr (e < 8 && (e & 1)) asm volatile("v_mul_f32 %0, %0, %3\n\tv_mul_f32 %1, %2, %4\n\tv_exp_f32 %0, %0" : "+v"(T.t0), "=&v"(T.t2) : "v"(T.t1), "v"(P[e >> 2][e & 3]), "v"(P[(e - 1) >> 2][(e - 1) & 3]));
    else if constexpr (e < 8) asm volatile("v_mul_f32 %0, %0, %4\n\tv_mul_f32 %1, %1, %5\n\tv_exp_f32 %0, %0\n\tv_cvt_pk_bf16_f32 %2, %3, %1" : "+v"(T.t0), "+v"(T.t1), "=&v"(Go[(e - 1) >> 1]) : "v"(T.t2), "v"(P[e >> 2][e & 3]), "v"(P[(e - 1) >> 2][(e - 1) & 3]));
    else asm volatile("v_mul_f32 %0, %0, %3\n\ts_nop 0\n\tv_cvt_pk_bf16_f32 %1, %2, %0" : "+v"(T.t1), "=&v"(Go[3]) : "v"(T.t2), "v"(P[1][3]));
}
template <int N> DI void wait_bar() { asm volatile("s_waitcnt vmcnt(%0)\n\ts_barrier" :: "n"(N) : "memory"); }
DI float dpp_add8(float v) {
    v += __builtin_bit_cast(float, __builtin_amdgcn_update_dpp(0, __builtin_bit_cast(int, v), 0xB1, 0xf, 0xf, true));
    v += __builtin_bit_cast(float, __builtin_amdgcn_update_dpp(0, __builtin_bit_cast(int, v), 0x4E, 0xf, 0xf, true));
    v += __builtin_bit_cast(float, __builtin_amdgcn_update_dpp(0, __builtin_bit_cast(int, v), 0x141, 0xf, 0xf, true));
    return v;
}

constexpr int HALF = 73728;
constexpr int XPITCH = 1184;
constexpr int n_burst(int G) { return (G == 0 || G == 27) ? 6 : 0; }
constexpr bool reg_dma(int G) { return G != 25 && G != 26 && 3 * G + 15 < 162; }
#define STAUX 16
#define ARR 2
constexpr bool has_store(int G) { return G >= 30 && G % 3 == 0; }
constexpr int vm_allowed(int G) {
    int need = 3 * G + 3; if (need > 161) need = 161;
    int after = 0; bool seen = false;
    for (int n = 0; n < 9; ++n) { if (seen) ++after; if (n == need) seen = true; }
    for (int g = 0; g < G; ++g) {
        for (int b = 0; b < n_burst(g); ++b) { int n = 3 * g + 9 + b; if (seen) ++after; if (n == need) seen = true; }
        for (int q = 0; q < 3; ++q) {
            if (reg_dma(g)) { int n = 3 * g + 15 + q; if (seen) ++after; if (n == need) seen = true; }
            if (q == 0 && has_store(g)) { if (seen) ++after; }
        }
    }
    return seen ? after : -1;
}

__global__ __launch_bounds__(512, 2) void k_mlp(const u16* __restrict__ x1, const u16* __restrict__ w3, const u16* __restrict__ w4, u16* __restrict__ part) {
    extern __shared__ __attribute__((aligned(16))) char smem[];
    const int tid = threadIdx.x, l = tid & 63;
    const int w = __builtin_amdgcn_readfirstlane(tid >> 6);
    const int b = blockIdx.x, xc = b & 7, idx = b >> 3;
    const int fc = 2 * (xc & 3) + (idx & 1), rt = (xc >> 2) * 16 + (idx >> 1);
    const int wi = w >> 2, wj = w & 3;
    LDSP char* lds = (LDSP char*)smem;
    STAMP(0);

    u32x4 xr[9];
    {
        u32x4 rx = mk_rsrc(x1 + (size_t)(rt * 64 + w * 8) * D, 8 * D * 2);
        unsigned vo = (l >> 3) * (D * 2) + (l & 7) * 16;
        sfor<0, 9>([&](auto I) { ld16<I.value * 128>(xr[I.value], rx, vo); });
    }
    const u32x4 r3 = mk_rsrc((const char*)w3 + fc * 1152 + (size_t)w * 8 * 9216, D * F * 2 - (fc * 1152 + w * 8 * 9216));
    const u32x4 r4 = mk_rsrc((const char*)w4 + (size_t)(fc * 576 + w * 8) * 1152, D * F * 2 - (fc * 576 + w * 8) * 1152);
    const unsigned vo3 = (l >> 3) * 9216 + ((l & 7) ^ ((((l >> 4) & 1) | ((w & 1) << 1)) << 1)) * 16;
    const unsigned vo4 = (l >> 3) * 1152 + ((l & 7) ^ (((l >> 4) & 3) << 1)) * 16;
    const unsigned ldsw = w * 1024;
    auto issue = [&](auto N) {
        constexpr int n = N.value, st = n / 9, kk = n % 9;
        if constexpr (st < 9) dma16(r3, ldsw + (st & 1) * HALF + kk * 8192, vo3, kk * 64 * 9216 + st * 128);
        else dma16(r4, ldsw + (st & 1) * HALF + kk * 8192, vo4, kk * 64 * 1152 + (st - 9) * 128);
    };
    sfor<0, 9>(issue);
    asm volatile("s_waitcnt vmcnt(9)" : "+v"(xr[0]), "+v"(xr[1]), "+v"(xr[2]), "+v"(xr[3]), "+v"(xr[4]), "+v"(xr[5]), "+v"(xr[6]), "+v"(xr[7]), "+v"(xr[8]));
    STAMP(1);
    {
        f32x2 v[36];
        f32x2 s2 = {0.f, 0.f};
#pragma unroll
        for (int i = 0; i < 36; ++i) { unsigned d = xr[i >> 2][i & 3]; v[i] = f32x2{bflo(d), bfhi(d)}; s2 += v[i]; }
        const float mean = dpp_add8(s2[0] + s2[1]) * (1.f / D);
        f32x2 q2 = {0.f, 0.f};
#pragma unroll
        for (int i = 0; i < 36; ++i) { v[i] -= mean; q2 += v[i] * v[i]; }
        const float var = dpp_add8(q2[0] + q2[1]) * (1.f / D);
        const float rs = GS * __builtin_amdgcn_rsqf(var + 1e-5f);
        LDSP char* xw = lds + HALF + (w * 8 + (l >> 3)) * XPITCH + (l & 7) * 16;
#pragma unroll
        for (int i = 0; i < 9; ++i) {
            u32x4 o;
#pragma unroll
            for (int e = 0; e < 4; ++e) { f32x2 t = v[i * 4 + e] * rs; o[e] = pk2(t[0], t[1]); }
            *(LDSP u32x4*)(xw + i * 128) = o;
        }
    }
    asm volatile("s_waitcnt lgkmcnt(0)\n\ts_barrier" ::: "memory");
    STAMP(2);
    bf16x8 X[36];
    {
        LDSP char* xb = lds + HALF + (wi * 32 + (l & 15)) * XPITCH + (l >> 4) * 16;
#pragma unroll
        for (int ks = 0; ks < 18; ++ks) { X[2 * ks] = *(LDSP bf16x8*)(xb + ks * 64); X[2 * ks + 1] = *(LDSP bf16x8*)(xb + 16 * XPITCH + ks * 64); }
    }
    const int g = l >> 4, q = (l >> 2) & 3, p = l & 3;
    const int sw = ((g & 1) << 1) | (q >> 1);
    LDSP char* tb1 = lds + (8 * g + q) * 128 + (wj ^ sw) * 32 + p * 8;
    LDSP char* tb2 = lds + (4 * g + q) * 128 + (wj ^ sw) * 32 + p * 8;
    auto ldw = [&](auto KS) -> bf16x8 {
        constexpr int st = KS.value / 18, ks = KS.value % 18;
        LDSP char* a = (st < 9 ? tb1 : tb2) + (st & 1) * HALF + ks * 4096;
        s16x4 lo = __builtin_amdgcn_ds_read_tr16_b64_v4i16((LDSP s16x4*)a);
        s16x4 hi = __builtin_amdgcn_ds_read_tr16_b64_v4i16((LDSP s16x4*)(a + (st < 9 ? 512 : 2048)));
        return __builtin_bit_cast(bf16x8, __builtin_shufflevector(lo, hi, 0, 1, 2, 3, 4, 5, 6, 7));
    };
    bf16x8 Wf[3];
    unsigned Gown[9][4];
    f32x4 acc[2][2];
    GeluT T;
    auto rp = __builtin_amdgcn_make_buffer_rsrc((void*)(part + ((size_t)(fc * 32 + rt) * 72 + w) * 512), 0, 65 * 1024, 0x00020000);

    auto store_part = [&](f32x4* P, int sd) {
        u32x4 o = {pk2(P[0][0], P[0][1]), pk2(P[0][2], P[0][3]), pk2(P[1][0], P[1][1]), pk2(P[1][2], P[1][3])};
        __builtin_amdgcn_raw_buffer_store_b128(o, rp, l * 16, sd * 8192, STAUX);
    };

    sfor<0, 18>([&](auto ST) {
        constexpr int st = ST.value;
        if constexpr (st == 9) {
            STAMP(21);
            asm volatile("s_nop 7" ::: "memory");
            sfor<0, 9>([&](auto E) { gelu_even<E.value>(T, acc[0]); asm volatile("s_nop 0"); gelu_odd<E.value>(T, acc[0], Gown[8]); asm volatile("s_nop 0"); });
            asm volatile("s_waitcnt lgkmcnt(0)\n\ts_barrier" ::: "memory");
            LDSP char* gw = lds + (wi * 36 + (wj >> 1)) * 1024 + l * 16 + (wj & 1) * 8;
#pragma unroll
            for (int s = 0; s < 9; ++s) {
                *(LDSP u32x2*)(gw + s * 2048) = u32x2{Gown[s][0], Gown[s][1]};
                *(LDSP u32x2*)(gw + s * 2048 + 18432) = u32x2{Gown[s][2], Gown[s][3]};
            }
            asm volatile("s_waitcnt lgkmcnt(0)\n\ts_barrier" ::: "memory");
            LDSP char* gr = lds + wi * 36864 + l * 16;
#pragma unroll
            for (int ks = 0; ks < 18; ++ks) { X[2 * ks] = *(LDSP bf16x8*)(gr + ks * 1024); X[2 * ks + 1] = *(LDSP bf16x8*)(gr + 18432 + ks * 1024); }
        }
        sfor<0, 18>([&](auto KS) {
            constexpr int ks = KS.value, gk = st * 18 + ks, G = gk / 6;
            if constexpr (ks % 6 == 0) {
                if constexpr (n_burst(G) > 0) asm volatile("s_waitcnt lgkmcnt(0)" ::: "memory");
                wait_bar<vm_allowed(G)>();
                if constexpr (ks == 0) STAMP(3 + st);
                if constexpr (n_burst(G) > 0) sfor<3 * G + 9, 3 * G + 15>(issue);
                if constexpr (gk == 0) { Wf[0] = ldw(std::integral_constant<int, 0>{}); Wf[1] = ldw(std::integral_constant<int, 1>{}); }
                SB();
            }
            f32x4* A = acc[st & 1];
            f32x4* P = acc[(st & 1) ^ 1];
            mfma2<ks == 0>(A[0], A[1], Wf[gk % 3], X[2 * ks], X[2 * ks + 1]);
            SB();
            if constexpr (gk + 2 < 324) Wf[(gk + 2) % 3] = ldw(std::integral_constant<int, gk + 2>{});
            SB();
            if constexpr (st >= 1 && st <= 8) {
                if constexpr ((ks & 1) == 0) gelu_even<ks / 2>(T, P);
                else gelu_odd<ks / 2>(T, P, Gown[st - 1]);
            }
            if constexpr (st >= 10 && ks == 1) store_part(P, st - 10);
            if constexpr ((ks & 1) == 0 && reg_dma(G)) issue(std::integral_constant<int, 3 * G + 15 + (ks % 6) / 2>{});
            SB();
        });
    });
    asm volatile("s_nop 7\n\ts_nop 1" : "+v"(acc[1][0]), "+v"(acc[1][1]));
    store_part(acc[1], 8);
}

__global__ __launch_bounds__(256) void k_fin(const u16* __restrict__ part, const u16* __restrict__ x1, const u16* __restrict__ yT,
                                             const u16* __restrict__ q, const float* __restrict__ gate, u16* __restrict__ out) {
    const int l = threadIdx.x & 63;
    const int piece = blockIdx.x * 4 + (threadIdx.x >> 6);
    const int rt = piece / 72, sd = (piece % 72) >> 3, w = piece & 7;
    u32x4 v[8];
#pragma unroll
    for (int fc = 0; fc < 8; ++fc) v[fc] = *(const u32x4*)(part + ((size_t)(fc * 32) * 72 + piece) * 512 + l * 8);
    const int col = sd * 64 + (w & 3) * 16 + (l >> 4) * 4;
    u32x2 xv[2], qv[2]; u16 yv[2][4]; float gv[2];
#pragma unroll
    for (int t = 0; t < 2; ++t) {
        const int row = rt * 64 + (w >> 2) * 32 + t * 16 + (l & 15);
        xv[t] = *(const u32x2*)(x1 + row * D + col); qv[t] = *(const u32x2*)(q + row * D + col); gv[t] = gate[row];
#pragma unroll
        for (int e = 0; e < 4; ++e) yv[t][e] = yT[(col + e) * S + row];
    }
    float s[8] = {};
#pragma unroll
    for (int fc = 0; fc < 8; ++fc)
#pragma unroll
        for (int e = 0; e < 4; ++e) { s[2 * e] += bflo(v[fc][e]); s[2 * e + 1] += bfhi(v[fc][e]); }
#pragma unroll
    for (int t = 0; t < 2; ++t) {
        const int row = rt * 64 + (w >> 2) * 32 + t * 16 + (l & 15);
        float o[4];
#pragma unroll
        for (int e = 0; e < 4; ++e) {
            const float xx = (e & 1) ? bfhi(xv[t][e >> 1]) : bflo(xv[t][e >> 1]);
            const float qq = (e & 1) ? bfhi(qv[t][e >> 1]) : bflo(qv[t][e >> 1]);
            o[e] = gv[t] * (qq * bf2f(yv[t][e])) + rbf(1.f - gv[t]) * (xx + s[t * 4 + e] * GINV);
        }
        *(u32x2*)(out + row * D + col) = u32x2{pk2(o[0], o[1]), pk2(o[2], o[3])};
    }
}

constexpr int UP = 2056;
DI bf16x8 ld_tr(LDSP char* a, int second) {
    s16x4 lo = __builtin_amdgcn_ds_read_tr16_b64_v4i16((LDSP s16x4*)a);
    s16x4 hi = __builtin_amdgcn_ds_read_tr16_b64_v4i16((LDSP s16x4*)(a + second));
    return __builtin_bit_cast(bf16x8, __builtin_shufflevector(lo, hi, 0, 1, 2, 3, 4, 5, 6, 7));
}
DI float gelu_f(float x) {
    const float u = x * GS;
    const float e = __builtin_amdgcn_exp2f(u * __builtin_fmaf(-u, u, GALPHA));
    return x * __builtin_amdgcn_rcpf(1.f + e);
}
DI int wrap9(int v) { return v >= 9 ? v - 9 : v; }

__global__ __launch_bounds__(576) void k_front1(const u16* __restrict__ x, const u16* __restrict__ w1, const u16* __restrict__ gw,
                                                const u16* __restrict__ k, const u16* __restrict__ v, const u16* __restrict__ shk,
                                                u16* __restrict__ h, float* __restrict__ gate, u16* __restrict__ uT) {
    extern __shared__ __attribute__((aligned(16))) char smem[];
    LDSP char* lds = (LDSP char*)smem;
    const int tid = threadIdx.x, l = tid & 63;
    const int w = __builtin_amdgcn_readfirstlane(tid >> 6);
    const int xc = blockIdx.x & 7, idx = blockIdx.x >> 3;
    if (idx < 24) {
        if (w == 8) return;
        const int rt = 4 * xc + idx / 6, cb = idx % 6;
        constexpr int WOFF = 64 * XPITCH;
        u32x4 xr[9];
        {
            u32x4 rx = mk_rsrc(x + (size_t)(rt * 64 + w * 8) * D, 8 * D * 2);
            unsigned vo = (l >> 3) * (D * 2) + (l & 7) * 16;
            sfor<0, 9>([&](auto I) { ld16<I.value * 128>(xr[I.value], rx, vo); });
        }
        const int kb0 = (idx / 6) * 2;
        const u32x4 rw = mk_rsrc(w1, D * D * 2);
        const unsigned vow = ((w & 3) * 16 + (l >> 2)) * 1152 + cb * 192 + ((l & 3) ^ ((l >> 5) << 1)) * 16;
        auto issue = [&](int i) {
            const int kb = wrap9(kb0 + i);
            const unsigned dst = WOFF + (i % 7) * 12288 + (w & 3) * 1024;
            dma16(rw, dst, vow, kb * 73728); dma16(rw, dst + 4096, vow, kb * 73728 + 64); dma16(rw, dst + 8192, vow, kb * 73728 + 128);
        };
        if (w < 4) {
#pragma unroll
            for (int i = 0; i < 7; ++i) issue(i);
            asm volatile("s_waitcnt vmcnt(21)" ::: "memory");
        } else {
            asm volatile("s_waitcnt vmcnt(0)" ::: "memory");
        }
        asm volatile("" : "+v"(xr[0]), "+v"(xr[1]), "+v"(xr[2]), "+v"(xr[3]), "+v"(xr[4]), "+v"(xr[5]), "+v"(xr[6]), "+v"(xr[7]), "+v"(xr[8]));
        {
            f32x2 vv[36];
            f32x2 s2 = {0.f, 0.f};
#pragma unroll
            for (int i = 0; i < 36; ++i) { unsigned d = xr[i >> 2][i & 3]; vv[i] = f32x2{bflo(d), bfhi(d)}; s2 += vv[i]; }
            const float mean = dpp_add8(s2[0] + s2[1]) * (1.f / D);
            f32x2 q2 = {0.f, 0.f};
#pragma unroll
            for (int i = 0; i < 36; ++i) { vv[i] -= mean; q2 += vv[i] * vv[i]; }
            const float var = dpp_add8(q2[0] + q2[1]) * (1.f / D);
            const float rs = __builtin_amdgcn_rsqf(var + 1e-5f);
            LDSP char* xw = lds + (w * 8 + (l >> 3)) * XPITCH + (l & 7) * 16;
#pragma unroll
            for (int i = 0; i < 9; ++i) {
                u32x4 o;
#pragma unroll
                for (int e = 0; e < 4; ++e) { f32x2 t = vv[i * 4 + e] * rs; o[e] = pk2(t[0], t[1]); }
                *(LDSP u32x4*)(xw + i * 128) = o;
            }
        }
        const int r4 = w & 3, ch = w >> 2;
        const int g = l >> 4, q = (l >> 2) & 3, p = l & 3;
        LDSP char* xb = lds + (r4 * 16 + (l & 15)) * XPITCH + g * 16;
        LDSP char* wb[3];
#pragma unroll
        for (int c = 0; c < 3; ++c) { const int ct = ch * 3 + c; wb[c] = lds + WOFF + (ct >> 1) * 4096 + (8 * g + q) * 64 + ((ct & 1) ^ (g & 1)) * 32 + p * 8; }
        f32x4 acc[3] = {};
        sfor<0, 9>([&](auto I) {
            constexpr int i = I.value;
            constexpr int issued = i <= 1 ? 7 : i == 2 ? 8 : 9;
            asm volatile("s_waitcnt lgkmcnt(0)" ::: "memory");
            wait_bar<3 * (issued - 1 - i)>();
            if constexpr (i == 1 || i == 2) { if (w < 4) issue(i + 6); }
            const int kb = wrap9(kb0 + i);
#pragma unroll
            for (int s = 0; s < 2; ++s) {
                bf16x8 xf = *(LDSP bf16x8*)(xb + kb * 128 + s * 64);
#pragma unroll
                for (int c = 0; c < 3; ++c) {
                    bf16x8 wf = ld_tr(wb[c] + (i % 7) * 12288 + s * 2048, 256);
                    acc[c] = __builtin_amdgcn_mfma_f32_16x16x32_bf16(wf, xf, acc[c], 0, 0, 0);
                }
            }
        });
        u16* ho = h + (size_t)(rt * 64 + r4 * 16 + (l & 15)) * D + cb * 96 + ch * 48 + g * 4;
#pragma unroll
        for (int c = 0; c < 3; ++c)
            *(u32x2*)(ho + c * 16) = u32x2{pk2(gelu_f(acc[c][0]), gelu_f(acc[c][1])), pk2(gelu_f(acc[c][2]), gelu_f(acc[c][3]))};
        if (cb == 0) {
            float gs = 0.f;
#pragma unroll
            for (int i = 0; i < 9; ++i) {
                const u32x4 gv = *(const u32x4*)(gw + i * 64 + (l & 7) * 8);
#pragma unroll
                for (int e = 0; e < 4; ++e) { const unsigned pr = pk2(bflo(xr[i][e]) * bflo(gv[e]), bfhi(xr[i][e]) * bfhi(gv[e])); gs += bflo(pr) + bfhi(pr); }
            }
            gs = rbf(dpp_add8(gs));
            if ((l & 7) == 0) gate[rt * 64 + w * 8 + (l >> 3)] = rbf(__builtin_amdgcn_rcpf(1.f + __builtin_amdgcn_exp2f(-1.44269504f * gs)));
        }
    } else {
        const int pb = xc * 8 + idx - 24, tb = pb >> 1, c0 = (pb & 1) * 288;
        const int rg = tid / 36, c = tid % 36;
        const int t0 = tb * 64 + rg * 4, chn = c0 + c * 8;
        auto rk = __builtin_amdgcn_make_buffer_rsrc((void*)k, 0, S * D * 2, 0x00020000);
        u32x4 kr[7], vr[4], sk[4];
#pragma unroll
        for (int i = 0; i < 7; ++i) kr[i] = __builtin_amdgcn_raw_buffer_load_b128(rk, (i < 3 && t0 == 0) ? 0x40000000 : ((t0 - 3 + i) * D + chn) * 2, 0, 0);
#pragma unroll
        for (int i = 0; i < 4; ++i) vr[i] = *(const u32x4*)(v + (size_t)(t0 + i) * D + chn);
#pragma unroll
        for (int i = 0; i < 4; ++i) sk[i] = *(const u32x4*)(shk + chn * 4 + i * 8);
        LDSP char* tw = lds + c * 1040 + (15 - rg) * 8;
#pragma unroll
        for (int e = 0; e < 8; ++e) {
            const float s0 = bflo(sk[e >> 1][(e & 1) * 2]), s1 = bfhi(sk[e >> 1][(e & 1) * 2]), s2 = bflo(sk[e >> 1][(e & 1) * 2 + 1]), s3 = bfhi(sk[e >> 1][(e & 1) * 2 + 1]);
            float kk[7], u[4];
#pragma unroll
            for (int i = 0; i < 7; ++i) kk[i] = (e & 1) ? bfhi(kr[i][e >> 1]) : bflo(kr[i][e >> 1]);
#pragma unroll
            for (int i = 0; i < 4; ++i) {
                const float ks = kk[i] * s3 + kk[i + 1] * s2 + kk[i + 2] * s1 + kk[i + 3] * s0;
                u[i] = ks * ((e & 1) ? bfhi(vr[i][e >> 1]) : bflo(vr[i][e >> 1]));
            }
            *(LDSP u32x2*)(tw + e * 128) = u32x2{pk2(u[3], u[2]), pk2(u[1], u[0])};
        }
        __syncthreads();
#pragma unroll
        for (int i = 0; i < 4; ++i) {
            const int pc = tid + i * 576, cl = pc >> 3, pp = pc & 7;
            const u32x4 o = *(LDSP u32x4*)(lds + cl * 128 + (cl >> 3) * 16 + pp * 16);
            *(u32x4*)(uT + (size_t)(c0 + cl) * UP + (1984 - tb * 64) + pp * 8) = o;
        }
        if (tb == 0 && tid < 288) *(u32x4*)(uT + (size_t)(c0 + tid) * UP + 2048) = u32x4{0u, 0u, 0u, 0u};
    }
}

constexpr int CV_CH = 4 * 4160 + 2 * 4576;
__global__ __launch_bounds__(512) void k_front2(const u16* __restrict__ x, const u16* __restrict__ h, const u16* __restrict__ w2,
                                                const u16* __restrict__ uT, const u16* __restrict__ ssk, const u16* __restrict__ ssD,
                                                u16* __restrict__ x1, u16* __restrict__ yT) {
    extern __shared__ __attribute__((aligned(16))) char smem[];
    LDSP char* lds = (LDSP char*)smem;
    const int tid = threadIdx.x, l = tid & 63;
    const int w = __builtin_amdgcn_readfirstlane(tid >> 6);
    const int xc = blockIdx.x & 7, idx = blockIdx.x >> 3;
    const int g = l >> 4;
    if (idx < 12) {
        const int rt = 4 * xc + idx / 3, cb = idx % 3;
        const int kb0 = (idx / 3) * 2;
        const u32x4 rw = mk_rsrc(w2, D * D * 2), rh = mk_rsrc(h, S * D * 2);
        const unsigned vow = ((w & 3) * 16 + (l >> 2)) * 1152 + cb * 384 + (w >> 2) * 192 + ((l & 3) ^ ((l >> 5) << 1)) * 16;
        const int xrow = w * 8 + (l >> 3), xch = (l & 7) ^ ((xrow >> 1) & 7);
        auto issue = [&](int i) {
            const int kb = wrap9(kb0 + i);
            const int ck = kb * 8 + xch;
            const int sh = ck < 18 ? -1 : ck < 36 ? 1 : ck < 54 ? -2 : 2;
            const unsigned vox = (unsigned)((rt * 64 + xrow + sh) * 1152 + ck * 16);
            const unsigned slot = (i % 5) * 32768;
            dma16(rh, slot + w * 1024, vox, 0);
            const unsigned dst = slot + 8192 + (w >> 2) * 12288 + (w & 3) * 1024;
            dma16(rw, dst, vow, kb * 73728); dma16(rw, dst + 4096, vow, kb * 73728 + 64); dma16(rw, dst + 8192, vow, kb * 73728 + 128);
        };
#pragma unroll
        for (int i = 0; i < 5; ++i) issue(i);
        const int rg = w >> 2, cq = w & 3;
        const int q = (l >> 2) & 3, p = l & 3, r = l & 15;
        LDSP char* xb = lds + (rg * 32 + r) * 128;
        const int xs = (r >> 1) & 7;
        LDSP char* wb[3];
#pragma unroll
        for (int c = 0; c < 3; ++c) { const int ct = cq * 3 + c; wb[c] = lds + 8192 + (ct >> 1) * 4096 + (8 * g + q) * 64 + ((ct & 1) ^ (g & 1)) * 32 + p * 8; }
        f32x4 acc[2][3] = {};
        sfor<0, 9>([&](auto I) {
            constexpr int i = I.value;
            constexpr int issued = i == 0 ? 5 : (i + 4 > 9 ? 9 : i + 4);
            asm volatile("s_waitcnt lgkmcnt(0)" ::: "memory");
            wait_bar<4 * (issued - 1 - i)>();
            if constexpr (i >= 1 && i + 4 < 9) issue(i + 4);
#pragma unroll
            for (int s = 0; s < 2; ++s) {
                bf16x8 xf0 = *(LDSP bf16x8*)(xb + (i % 5) * 32768 + (((s * 4 + g) ^ xs) * 16));
                bf16x8 xf1 = *(LDSP bf16x8*)(xb + (i % 5) * 32768 + 2048 + (((s * 4 + g) ^ xs) * 16));
#pragma unroll
                for (int c = 0; c < 3; ++c) {
                    bf16x8 wf = ld_tr(wb[c] + (i % 5) * 32768 + s * 2048, 256);
                    acc[0][c] = __builtin_amdgcn_mfma_f32_16x16x32_bf16(wf, xf0, acc[0][c], 0, 0, 0);
                    acc[1][c] = __builtin_amdgcn_mfma_f32_16x16x32_bf16(wf, xf1, acc[1][c], 0, 0, 0);
                }
            }
        });
        const size_t o0 = (size_t)(rt * 64 + rg * 32 + r) * D + cb * 192 + cq * 48 + g * 4;
        u32x2 xv[2][3];
#pragma unroll
        for (int t = 0; t < 2; ++t)
#pragma unroll
            for (int c = 0; c < 3; ++c) xv[t][c] = *(const u32x2*)(x + o0 + t * 16 * D + c * 16);
#pragma unroll
        for (int t = 0; t < 2; ++t)
#pragma unroll
            for (int c = 0; c < 3; ++c)
                *(u32x2*)(x1 + o0 + t * 16 * D + c * 16) = u32x2{pk2(bflo(xv[t][c][0]) + acc[t][c][0], bfhi(xv[t][c][0]) + acc[t][c][1]),
                                                               pk2(bflo(xv[t][c][1]) + acc[t][c][2], bfhi(xv[t][c][1]) + acc[t][c][3])};
    } else {
        const int cw = w & 3, ch = (xc * 18 + idx - 12) * 4 + cw;
        const unsigned cbase = cw * CV_CH;
        const u32x4 rk = mk_rsrc(ssk + (size_t)ch * S, S * 2), ru = mk_rsrc(uT + (size_t)ch * UP, UP * 2);
        if (w < 4) {
#pragma unroll
            for (int i = 0; i < 4; ++i) {
                dma16(ru, cbase + 16640 + i * 1024, l * 16 + 2, i * 1024);
                dma16(ru, cbase + 16640 + 4576 + i * 1024, l * 16, i * 1024);
                dma16(rk, cbase + 64 + i * 1024, l * 16 + 4, i * 1024);
            }
        } else {
            const u32x4 k0 = *(const u32x4*)(ssk + (size_t)ch * S);
#pragma unroll
            for (int i = 0; i < 4; ++i)
#pragma unroll
                for (int c = 1; c < 4; ++c) dma16(rk, cbase + c * 4160 + 64 + i * 1024, l * 16 + 4 + 4 * c, i * 1024);
            if (l < 16) {
                const int c = l >> 2;
                u32x4 o = {0u, 0u, 0u, 0u};
                if ((l & 3) == 3) {
                    o[3] = c == 0 ? k0[0] : c == 1 ? k0[1] : c == 2 ? k0[2] : k0[3];
                    o[2] = c == 0 ? 0u : c == 1 ? k0[0] : c == 2 ? k0[1] : k0[2];
                    o[1] = c <= 1 ? 0u : c == 2 ? k0[0] : k0[1];
                    o[0] = c <= 2 ? 0u : k0[0];
                }
                *(LDSP u32x4*)(lds + cbase + c * 4160 + (l & 3) * 16) = o;
            }
            if (l < 56) *(LDSP u32x4*)(lds + cbase + 16640 + (l / 28) * 4576 + 4096 + (l % 28) * 16) = u32x4{0u, 0u, 0u, 0u};
        }
        const unsigned dD = ssD[ch];
        asm volatile("s_waitcnt vmcnt(0) lgkmcnt(0)\n\ts_barrier" ::: "memory");
        if (w >= 4) return;
        const int r = l & 15;
        LDSP char* ab = lds + cbase + (r & 3) * 4160 + ((r >> 2) + g) * 16;
        LDSP char* bb = lds + cbase + 16640 + (r & 1) * 4576 + (252 - 4 * (r >> 1) + g) * 16;
        f32x4 acc[8] = {};
        bf16x8 A[2][8], B[2][8];
        auto ldf = [&](int t, bf16x8* a, bf16x8* b) {
#pragma unroll
            for (int m = 0; m < 8; ++m) { b[m] = *(LDSP bf16x8*)(bb - 3584 + (7 - m) * 512 + t * 64); a[m] = *(LDSP bf16x8*)(ab + (t + 8 * m) * 64); }
        };
        ldf(0, A[0], B[0]);
        {
            const int j = 30 - 2 * r - 8 * g;
            u32x4 dv;
#pragma unroll
            for (int e = 0; e < 4; ++e) dv[e] = (j == 2 * e) ? dD : 0u;
            const bf16x8 da = __builtin_bit_cast(bf16x8, dv);
#pragma unroll
            for (int n = 0; n < 8; ++n) acc[n] = __builtin_amdgcn_mfma_f32_16x16x32_bf16(da, B[0][n], acc[n], 0, 0, 0);
        }
#pragma unroll
        for (int t = 0; t < 8; ++t) {
            if (t < 7) ldf(t + 1, A[(t + 1) & 1], B[(t + 1) & 1]);
#pragma unroll
            for (int m = 0; m < 8; ++m)
#pragma unroll
                for (int n = 0; n + m < 8; ++n) acc[m + n] = __builtin_amdgcn_mfma_f32_16x16x32_bf16(A[t & 1][m], B[t & 1][n], acc[m + n], 0, 0, 0);
            if (t < 7) {
#pragma unroll
                for (int z = 0; z < 16; ++z) { __builtin_amdgcn_sched_group_barrier(0x8, 2, 0); __builtin_amdgcn_sched_group_barrier(0x100, 1, 0); }
            }
            SB();
        }
        u16* yo = yT + (size_t)ch * S + 32 * (r >> 1) + 8 * g + 4 * (r & 1);
#pragma unroll
        for (int c = 0; c < 8; ++c) {
            const bool odd = r & 1;
            const float s0 = odd ? acc[c][0] : acc[c][2], s1 = odd ? acc[c][1] : acc[c][3];
            const float r0 = __builtin_bit_cast(float, __builtin_amdgcn_update_dpp(0, __builtin_bit_cast(int, s0), 0xB1, 0xf, 0xf, true));
            const float r1 = __builtin_bit_cast(float, __builtin_amdgcn_update_dpp(0, __builtin_bit_cast(int, s1), 0xB1, 0xf, 0xf, true));
            const u32x2 o = odd ? u32x2{pk2(r0, acc[c][2]), pk2(r1, acc[c][3])} : u32x2{pk2(acc[c][0], r0), pk2(acc[c][1], r1)};
            *(u32x2*)(yo + c * 256) = o;
        }
    }
}

extern "C" void kernel_launch(void* const* d_in, const int* in_sizes, int n_in, void* d_out, int out_size, void* d_ws, size_t ws_size, hipStream_t stream) {
    const u16* q = (const u16*)d_in[0]; const u16* k = (const u16*)d_in[1]; const u16* v = (const u16*)d_in[2];
    const u16* shk = (const u16*)d_in[3]; const u16* ssk = (const u16*)d_in[4]; const u16* ssD = (const u16*)d_in[5];
    const u16* w1 = (const u16*)d_in[7]; const u16* w2 = (const u16*)d_in[8]; const u16* w3 = (const u16*)d_in[9]; const u16* w4 = (const u16*)d_in[10];
    const u16* gw = (const u16*)d_in[11];
    u16* ws = (u16*)d_ws;
    u16* uT = ws; float* gate = (float*)(ws + S * D + 16384); u16* yT = ws + 2 * S * D; u16* h = ws + 3 * S * D; u16* x1 = ws + 4 * S * D;
    u16* part = ws + 5 * S * D;
    (void)hipFuncSetAttribute((const void*)k_front1, hipFuncAttributeMaxDynamicSharedMemorySize, 163840);
    (void)hipFuncSetAttribute((const void*)k_front2, hipFuncAttributeMaxDynamicSharedMemorySize, 163840);
    (void)hipFuncSetAttribute((const void*)k_mlp, hipFuncAttributeMaxDynamicSharedMemorySize, 163840);
    k_front1<<<256, 576, 163840, stream>>>(q, w1, gw, k, v, shk, h, gate, uT);
    k_front2<<<240, 512, 163840, stream>>>(q, h, w2, uT, ssk, ssD, x1, yT);
    k_mlp<<<256, 512, 163840, stream>>>(x1, w3, w4, part);
    k_fin<<<576, 256, 0, stream>>>(part, x1, yT, q, gate, (u16*)d_out);
}
```

```cpp
#include <hip/hip_runtime.h>
#include <hip/hip_bf16.h>
#include <stdint.h>
#include <type_traits>

typedef unsigned short u16;
typedef __attribute__((ext_vector_type(4))) short s16x4;
typedef __attribute__((ext_vector_type(8))) __bf16 bf16x8;
typedef __attribute__((ext_vector_type(2))) __bf16 bf16x2;
typedef __attribute__((ext_vector_type(4))) float f32x4;
typedef __attribute__((ext_vector_type(2))) float f32x2;
typedef __attribute__((ext_vector_type(4))) unsigned u32x4;
typedef __attribute__((ext_vector_type(2))) unsigned u32x2;
#define LDSP __attribute__((address_space(3)))
#define DI __device__ __forceinline__
#define SB() __builtin_amdgcn_sched_barrier(0)

constexpr int S = 2048, D = 576, F = 4608;
constexpr float GS = 0.46866869f;
constexpr float GALPHA = -4.91222954f;
constexpr float GINV = 2.13370343f;

template <int I, int N, class Fn> DI void sfor(Fn&& f) {
    if constexpr (I < N) { f(std::integral_constant<int, I>{}); sfor<I + 1, N>(f); }
}
DI float bf2f(u16 v) { return __uint_as_float(((unsigned)v) << 16); }
DI float bflo(unsigned v) { return __uint_as_float(v << 16); }
DI float bfhi(unsigned v) { return __uint_as_float(v & 0xffff0000u); }
DI unsigned pk2(float a, float b) { f32x2 v = {a, b}; return __builtin_bit_cast(unsigned, __builtin_convertvector(v, bf16x2)); }
DI u16 f2bf(float f) { return (u16)(pk2(f, 0.f) & 0xffffu); }
DI float rbf(float f) { return bflo(pk2(f, 0.f)); }
DI u32x4 mk_rsrc(const void* p, unsigned bytes) {
    unsigned long long a = (unsigned long long)p;
    u32x4 r = {(unsigned)a, (unsigned)(a >> 32) & 0xffffu, bytes, 0x00020000u};
    return r;
}
DI void dma16(u32x4 rs, unsigned lds, unsigned voff, unsigned soff) {
    asm volatile("s_mov_b32 m0, %0\n\ts_nop 0\n\tbuffer_load_dwordx4 %1, %2, %3 offen lds" :: "s"(lds), "v"(voff), "s"(rs), "s"(soff) : "memory");
}
template <int OFF> DI void ld16(u32x4& d, u32x4 rs, unsigned vo) {
    asm volatile("buffer_load_dwordx4 %0, %1, %2, 0 offen offset:%3" : "=v"(d) : "v"(vo), "s"(rs), "n"(OFF));
}
template <bool FIRST> DI void mfma2(f32x4& c0, f32x4& c1, const bf16x8& a, const bf16x8& b0, const bf16x8& b1) {
    if constexpr (FIRST) asm volatile("v_mfma_f32_16x16x32_bf16 %0, %2, %3, 0\n\tv_mfma_f32_16x16x32_bf16 %1, %2, %4, 0" : "=&v"(c0), "=&v"(c1) : "v"(a), "v"(b0), "v"(b1));
    else asm volatile("v_mfma_f32_16x16x32_bf16 %0, %2, %3, %0\n\tv_mfma_f32_16x16x32_bf16 %1, %2, %4, %1" : "+v"(c0), "+v"(c1) : "v"(a), "v"(b0), "v"(b1));
}
template <bool FIRST> DI void mfma1(f32x4& c0, const bf16x8& a, const bf16x8& b0) {
    if constexpr (FIRST) asm volatile("v_mfma_f32_16x16x32_bf16 %0, %1, %2, 0" : "=&v"(c0) : "v"(a), "v"(b0));
    else asm volatile("v_mfma_f32_16x16x32_bf16 %0, %1, %2, %0" : "+v"(c0) : "v"(a), "v"(b0));
}
#define STAMP(i) do {} while (0)
#define DUMP(kid, n) do {} while (0)
#define DECL do {} while (0)
#define DRAIN() do {} while (0)
struct GeluT { float t0, t1, t2; };
template <int e> DI void gelu_even(GeluT& T, const f32x4* P) {
    if constexpr (e == 0) asm volatile("v_fma_f32 %0, -%1, %1, %2" : "=v"(T.t0) : "v"(P[0][0]), "s"(GALPHA));
    else if constexpr (e < 8) asm volatile("v_add_f32 %1, 1.0, %0\n\tv_fma_f32 %0, -%2, %2, %3\n\tv_rcp_f32 %1, %1" : "+v"(T.t0), "=&v"(T.t1) : "v"(P[e >> 2][e & 3]), "s"(GALPHA));
    else asm volatile("v_add_f32 %0, 1.0, %1\n\ts_nop 0\n\tv_rcp_f32 %0, %0" : "=&v"(T.t1) : "v"(T.t0));
}
template <int e> DI void gelu_odd(GeluT& T, const f32x4* P, unsigned* Go) {
    if constexpr (e == 0) asm volatile("v_mul_f32 %0, %0, %1\n\ts_nop 0\n\tv_exp_f32 %0, %0" : "+v"(T.t0) : "v"(P[0][0]));
    else if constexpr (e < 8 && (e & 1)) asm volatile("v_mul_f32 %0, %0, %3\n\tv_mul_f32 %1, %2, %4\n\tv_exp_f32 %0, %0" : "+v"(T.t0), "=&v"(T.t2) : "v"(T.t1), "v"(P[e >> 2][e & 3]), "v"(P[(e - 1) >> 2][(e - 1) & 3]));
    else if constexpr (e < 8) asm volatile("v_mul_f32 %0, %0, %4\n\tv_mul_f32 %1, %1, %5\n\tv_exp_f32 %0, %0\n\tv_cvt_pk_bf16_f32 %2, %3, %1" : "+v"(T.t0), "+v"(T.t1), "=&v"(Go[(e - 1) >> 1]) : "v"(T.t2), "v"(P[e >> 2][e & 3]), "v"(P[(e - 1) >> 2][(e - 1) & 3]));
    else asm volatile("v_mul_f32 %0, %0, %3\n\ts_nop 0\n\tv_cvt_pk_bf16_f32 %1, %2, %0" : "+v"(T.t1), "=&v"(Go[3]) : "v"(T.t2), "v"(P[1][3]));
}
template <int N> DI void wait_bar() { asm volatile("s_waitcnt vmcnt(%0)\n\ts_barrier" :: "n"(N) : "memory"); }
DI float dpp_add8(float v) {
    v += __builtin_bit_cast(float, __builtin_amdgcn_update_dpp(0, __builtin_bit_cast(int, v), 0xB1, 0xf, 0xf, true));
    v += __builtin_bit_cast(float, __builtin_amdgcn_update_dpp(0, __builtin_bit_cast(int, v), 0x4E, 0xf, 0xf, true));
    v += __builtin_bit_cast(float, __builtin_amdgcn_update_dpp(0, __builtin_bit_cast(int, v), 0x141, 0xf, 0xf, true));
    return v;
}

constexpr int HALF = 73728;
constexpr int XPITCH = 1184;
constexpr int n_burst(int G) { return (G == 0 || G == 27) ? 6 : 0; }
constexpr bool reg_dma(int G) { return G != 25 && G != 26 && 3 * G + 15 < 162; }
#define STAUX 16
#define ARR 2
constexpr bool has_store(int G) { return G >= 30 && G % 3 == 0; }
constexpr int vm_allowed(int G) {
    int need = 3 * G + 3; if (need > 161) need = 161;
    int after = 0; bool seen = false;
    for (int n = 0; n < 9; ++n) { if (seen) ++after; if (n == need) seen = true; }
    for (int g = 0; g < G; ++g) {
        for (int b = 0; b < n_burst(g); ++b) { int n = 3 * g + 9 + b; if (seen) ++after; if (n == need) seen = true; }
        for (int q = 0; q < 3; ++q) {
            if (reg_dma(g)) { int n = 3 * g + 15 + q; if (seen) ++after; if (n == need) seen = true; }
            if (q == 0 && has_store(g)) { if (seen) ++after; }
        }
    }
    return seen ? after : -1;
}

__global__ __launch_bounds__(512, 2) void k_mlp(const u16* __restrict__ x1, const u16* __restrict__ w3, const u16* __restrict__ w4, u16* __restrict__ part) {
    extern __shared__ __attribute__((aligned(16))) char smem[];
    const int tid = threadIdx.x, l = tid & 63;
    const int w = __builtin_amdgcn_readfirstlane(tid >> 6);
    const int b = blockIdx.x, xc = b & 7, idx = b >> 3;
    const int fc = 2 * (xc & 3) + (idx & 1), rt = (xc >> 2) * 16 + (idx >> 1);
    const int wi = w >> 2, wj = w & 3;
    LDSP char* lds = (LDSP char*)smem;
    STAMP(0);

    u32x4 xr[9];
    {
        u32x4 rx = mk_rsrc(x1 + (size_t)(rt * 64 + w * 8) * D, 8 * D * 2);
        unsigned vo = (l >> 3) * (D * 2) + (l & 7) * 16;
        sfor<0, 9>([&](auto I) { ld16<I.value * 128>(xr[I.value], rx, vo); });
    }
    const u32x4 r3 = mk_rsrc((const char*)w3 + fc * 1152 + (size_t)w * 8 * 9216, D * F * 2 - (fc * 1152 + w * 8 * 9216));
    const u32x4 r4 = mk_rsrc((const char*)w4 + (size_t)(fc * 576 + w * 8) * 1152, D * F * 2 - (fc * 576 + w * 8) * 1152);
    const unsigned vo3 = (l >> 3) * 9216 + ((l & 7) ^ ((((l >> 4) & 1) | ((w & 1) << 1)) << 1)) * 16;
    const unsigned vo4 = (l >> 3) * 1152 + ((l & 7) ^ (((l >> 4) & 3) << 1)) * 16;
    const unsigned ldsw = w * 1024;
    auto issue = [&](auto N) {
        constexpr int n = N.value, st = n / 9, kk = n % 9;
        if constexpr (st < 9) dma16(r3, ldsw + (st & 1) * HALF + kk * 8192, vo3, kk * 64 * 9216 + st * 128);
        else dma16(r4, ldsw + (st & 1) * HALF + kk * 8192, vo4, kk * 64 * 1152 + (st - 9) * 128);
    };
    sfor<0, 9>(issue);
    asm volatile("s_waitcnt vmcnt(9)" : "+v"(xr[0]), "+v"(xr[1]), "+v"(xr[2]), "+v"(xr[3]), "+v"(xr[4]), "+v"(xr[5]), "+v"(xr[6]), "+v"(xr[7]), "+v"(xr[8]));
    STAMP(1);
    {
        f32x2 v[36];
        f32x2 s2 = {0.f, 0.f};
#pragma unroll
        for (int i = 0; i < 36; ++i) { unsigned d = xr[i >> 2][i & 3]; v[i] = f32x2{bflo(d), bfhi(d)}; s2 += v[i]; }
        const float mean = dpp_add8(s2[0] + s2[1]) * (1.f / D);
        f32x2 q2 = {0.f, 0.f};
#pragma unroll
        for (int i = 0; i < 36; ++i) { v[i] -= mean; q2 += v[i] * v[i]; }
        const float var = dpp_add8(q2[0] + q2[1]) * (1.f / D);
        const float rs = GS * __builtin_amdgcn_rsqf(var + 1e-5f);
        LDSP char* xw = lds + HALF + (w * 8 + (l >> 3)) * XPITCH + (l & 7) * 16;
#pragma unroll
        for (int i = 0; i < 9; ++i) {
            u32x4 o;
#pragma unroll
            for (int e = 0; e < 4; ++e) { f32x2 t = v[i * 4 + e] * rs; o[e] = pk2(t[0], t[1]); }
            *(LDSP u32x4*)(xw + i * 128) = o;
        }
    }
    asm volatile("s_waitcnt lgkmcnt(0)\n\ts_barrier" ::: "memory");
    STAMP(2);
    bf16x8 X[36];
    {
        LDSP char* xb = lds + HALF + (wi * 32 + (l & 15)) * XPITCH + (l >> 4) * 16;
#pragma unroll
        for (int ks = 0; ks < 18; ++ks) { X[2 * ks] = *(LDSP bf16x8*)(xb + ks * 64); X[2 * ks + 1] = *(LDSP bf16x8*)(xb + 16 * XPITCH + ks * 64); }
    }
    const int g = l >> 4, q = (l >> 2) & 3, p = l & 3;
    const int sw = ((g & 1) << 1) | (q >> 1);
    LDSP char* tb1 = lds + (8 * g + q) * 128 + (wj ^ sw) * 32 + p * 8;
    LDSP char* tb2 = lds + (4 * g + q) * 128 + (wj ^ sw) * 32 + p * 8;
    auto ldw = [&](auto KS) -> bf16x8 {
        constexpr int st = KS.value / 18, ks = KS.value % 18;
        LDSP char* a = (st < 9 ? tb1 : tb2) + (st & 1) * HALF + ks * 4096;
        s16x4 lo = __builtin_amdgcn_ds_read_tr16_b64_v4i16((LDSP s16x4*)a);
        s16x4 hi = __builtin_amdgcn_ds_read_tr16_b64_v4i16((LDSP s16x4*)(a + (st < 9 ? 512 : 2048)));
        return __builtin_bit_cast(bf16x8, __builtin_shufflevector(lo, hi, 0, 1, 2, 3, 4, 5, 6, 7));
    };
    bf16x8 Wf[3];
    unsigned Gown[9][4];
    f32x4 acc[2][2];
    GeluT T;
    auto rp = __builtin_amdgcn_make_buffer_rsrc((void*)(part + ((size_t)(fc * 32 + rt) * 72 + w) * 512), 0, 65 * 1024, 0x00020000);

    auto store_part = [&](f32x4* P, int sd) {
        u32x4 o = {pk2(P[0][0], P[0][1]), pk2(P[0][2], P[0][3]), pk2(P[1][0], P[1][1]), pk2(P[1][2], P[1][3])};
        __builtin_amdgcn_raw_buffer_store_b128(o, rp, l * 16, sd * 8192, STAUX);
    };

    sfor<0, 18>([&](auto ST) {
        constexpr int st = ST.value;
        if constexpr (st == 9) {
            STAMP(21);
            asm volatile("s_nop 7" ::: "memory");
            sfor<0, 9>([&](auto E) { gelu_even<E.value>(T, acc[0]); asm volatile("s_nop 0"); gelu_odd<E.value>(T, acc[0], Gown[8]); asm volatile("s_nop 0"); });
            asm volatile("s_waitcnt lgkmcnt(0)\n\ts_barrier" ::: "memory");
            LDSP char* gw = lds + (wi * 36 + (wj >> 1)) * 1024 + l * 16 + (wj & 1) * 8;
#pragma unroll
            for (int s = 0; s < 9; ++s) {
                *(LDSP u32x2*)(gw + s * 2048) = u32x2{Gown[s][0], Gown[s][1]};
                *(LDSP u32x2*)(gw + s * 2048 + 18432) = u32x2{Gown[s][2], Gown[s][3]};
            }
            asm volatile("s_waitcnt lgkmcnt(0)\n\ts_barrier" ::: "memory");
            LDSP char* gr = lds + wi * 36864 + l * 16;
#pragma unroll
            for (int ks = 0; ks < 18; ++ks) { X[2 * ks] = *(LDSP bf16x8*)(gr + ks * 1024); X[2 * ks + 1] = *(LDSP bf16x8*)(gr + 18432 + ks * 1024); }
        }
        sfor<0, 18>([&](auto KS) {
            constexpr int ks = KS.value, gk = st * 18 + ks, G = gk / 6;
            if constexpr (ks % 6 == 0) {
                if constexpr (n_burst(G) > 0) asm volatile("s_waitcnt lgkmcnt(0)" ::: "memory");
                wait_bar<vm_allowed(G)>();
                if constexpr (ks == 0) STAMP(3 + st);
                if constexpr (n_burst(G) > 0) sfor<3 * G + 9, 3 * G + 15>(issue);
                if constexpr (gk == 0) { Wf[0] = ldw(std::integral_constant<int, 0>{}); Wf[1] = ldw(std::integral_constant<int, 1>{}); }
                SB();
            }
            f32x4* A = acc[st & 1];
            f32x4* P = acc[(st & 1) ^ 1];
            mfma2<ks == 0>(A[0], A[1], Wf[gk % 3], X[2 * ks], X[2 * ks + 1]);
            SB();
            if constexpr (gk + 2 < 324) Wf[(gk + 2) % 3] = ldw(std::integral_constant<int, gk + 2>{});
            SB();
            if constexpr (st >= 1 && st <= 8) {
                if constexpr ((ks & 1) == 0) gelu_even<ks / 2>(T, P);
                else gelu_odd<ks / 2>(T, P, Gown[st - 1]);
            }
            if constexpr (st >= 10 && ks == 1) store_part(P, st - 10);
            if constexpr ((ks & 1) == 0 && reg_dma(G)) issue(std::integral_constant<int, 3 * G + 15 + (ks % 6) / 2>{});
            SB();
        });
    });
    asm volatile("s_nop 7\n\ts_nop 1" : "+v"(acc[1][0]), "+v"(acc[1][1]));
    store_part(acc[1], 8);
}

__global__ __launch_bounds__(256) void k_fin(const u16* __restrict__ part, const u16* __restrict__ x1, const u16* __restrict__ yT,
                                             const u16* __restrict__ q, const float* __restrict__ gate, u16* __restrict__ out) {
    const int l = threadIdx.x & 63;
    const int piece = blockIdx.x * 4 + (threadIdx.x >> 6);
    const int rt = piece / 72, sd = (piece % 72) >> 3, w = piece & 7;
    u32x4 v[8];
#pragma unroll
    for (int fc = 0; fc < 8; ++fc) v[fc] = *(const u32x4*)(part + ((size_t)(fc * 32) * 72 + piece) * 512 + l * 8);
    const int col = sd * 64 + (w & 3) * 16 + (l >> 4) * 4;
    u32x2 xv[2], qv[2]; u16 yv[2][4]; float gv[2];
#pragma unroll
    for (int t = 0; t < 2; ++t) {
        const int row = rt * 64 + (w >> 2) * 32 + t * 16 + (l & 15);
        xv[t] = *(const u32x2*)(x1 + row * D + col); qv[t] = *(const u32x2*)(q + row * D + col); gv[t] = gate[row];
#pragma unroll
        for (int e = 0; e < 4; ++e) yv[t][e] = yT[(col + e) * S + row];
    }
    float s[8] = {};
#pragma unroll
    for (int fc = 0; fc < 8; ++fc)
#pragma unroll
        for (int e = 0; e < 4; ++e) { s[2 * e] += bflo(v[fc][e]); s[2 * e + 1] += bfhi(v[fc][e]); }
#pragma unroll
    for (int t = 0; t < 2; ++t) {
        const int row = rt * 64 + (w >> 2) * 32 + t * 16 + (l & 15);
        float o[4];
#pragma unroll
        for (int e = 0; e < 4; ++e) {
            const float xx = (e & 1) ? bfhi(xv[t][e >> 1]) : bflo(xv[t][e >> 1]);
            const float qq = (e & 1) ? bfhi(qv[t][e >> 1]) : bflo(qv[t][e >> 1]);
            o[e] = gv[t] * (qq * bf2f(yv[t][e])) + rbf(1.f - gv[t]) * (xx + s[t * 4 + e] * GINV);
        }
        *(u32x2*)(out + row * D + col) = u32x2{pk2(o[0], o[1]), pk2(o[2], o[3])};
    }
}

constexpr int UP = 2056;
DI bf16x8 ld_tr(LDSP char* a, int second) {
    s16x4 lo = __builtin_amdgcn_ds_read_tr16_b64_v4i16((LDSP s16x4*)a);
    s16x4 hi = __builtin_amdgcn_ds_read_tr16_b64_v4i16((LDSP s16x4*)(a + second));
    return __builtin_bit_cast(bf16x8, __builtin_shufflevector(lo, hi, 0, 1, 2, 3, 4, 5, 6, 7));
}
DI float gelu_f(float x) {
    const float u = x * GS;
    const float e = __builtin_amdgcn_exp2f(u * __builtin_fmaf(-u, u, GALPHA));
    return x * __builtin_amdgcn_rcpf(1.f + e);
}
DI int wrap9(int v) { return v >= 9 ? v - 9 : v; }

__global__ __launch_bounds__(576) void k_front1(const u16* __restrict__ x, const u16* __restrict__ w1, const u16* __restrict__ gw,
                                                const u16* __restrict__ k, const u16* __restrict__ v, const u16* __restrict__ shk,
                                                u16* __restrict__ h, float* __restrict__ gate, u16* __restrict__ uT) {
    extern __shared__ __attribute__((aligned(16))) char smem[];
    LDSP char* lds = (LDSP char*)smem;
    const int tid = threadIdx.x, l = tid & 63;
    const int w = __builtin_amdgcn_readfirstlane(tid >> 6);
    const int xc = blockIdx.x & 7, idx = blockIdx.x >> 3;
    DECL; STAMP(0);
    if (idx < 24) {
        if (w == 8) return;
        const int rt = 4 * xc + idx / 6, cb = idx % 6;
        constexpr int WOFF = 64 * XPITCH;
        u32x4 xr[9];
        {
            u32x4 rx = mk_rsrc(x + (size_t)(rt * 64 + w * 8) * D, 8 * D * 2);
            unsigned vo = (l >> 3) * (D * 2) + (l & 7) * 16;
            sfor<0, 9>([&](auto I) { ld16<I.value * 128>(xr[I.value], rx, vo); });
        }
        const int kb0 = (idx / 6) * 2;
        const u32x4 rw = mk_rsrc(w1, D * D * 2);
        const unsigned vow = ((w & 3) * 16 + (l >> 2)) * 1152 + cb * 192 + ((l & 3) ^ ((l >> 5) << 1)) * 16;
        auto issue = [&](int i) {
            const int kb = wrap9(kb0 + i);
            const unsigned dst = WOFF + (i % 7) * 12288 + (w & 3) * 1024;
            dma16(rw, dst, vow, kb * 73728); dma16(rw, dst + 4096, vow, kb * 73728 + 64); dma16(rw, dst + 8192, vow, kb * 73728 + 128);
        };
        if (w < 4) {
#pragma unroll
            for (int i = 0; i < 7; ++i) issue(i);
            asm volatile("s_waitcnt vmcnt(21)" ::: "memory");
        } else {
            asm volatile("s_waitcnt vmcnt(0)" ::: "memory");
        }
        asm volatile("" : "+v"(xr[0]), "+v"(xr[1]), "+v"(xr[2]), "+v"(xr[3]), "+v"(xr[4]), "+v"(xr[5]), "+v"(xr[6]), "+v"(xr[7]), "+v"(xr[8]));
        STAMP(1);
        {
            f32x2 vv[36];
            f32x2 s2 = {0.f, 0.f};
#pragma unroll
            for (int i = 0; i < 36; ++i) { unsigned d = xr[i >> 2][i & 3]; vv[i] = f32x2{bflo(d), bfhi(d)}; s2 += vv[i]; }
            const float mean = dpp_add8(s2[0] + s2[1]) * (1.f / D);
            f32x2 q2 = {0.f, 0.f};
#pragma unroll
            for (int i = 0; i < 36; ++i) { vv[i] -= mean; q2 += vv[i] * vv[i]; }
            const float var = dpp_add8(q2[0] + q2[1]) * (1.f / D);
            const float rs = __builtin_amdgcn_rsqf(var + 1e-5f);
            LDSP char* xw = lds + (w * 8 + (l >> 3)) * XPITCH + (l & 7) * 16;
#pragma unroll
            for (int i = 0; i < 9; ++i) {
                u32x4 o;
#pragma unroll
                for (int e = 0; e < 4; ++e) { f32x2 t = vv[i * 4 + e] * rs; o[e] = pk2(t[0], t[1]); }
                *(LDSP u32x4*)(xw + i * 128) = o;
            }
        }
        const int r4 = w & 3, ch = w >> 2;
        const int g = l >> 4, q = (l >> 2) & 3, p = l & 3;
        LDSP char* xb = lds + (r4 * 16 + (l & 15)) * XPITCH + g * 16;
        LDSP char* wb[3];
#pragma unroll
        for (int c = 0; c < 3; ++c) { const int ct = ch * 3 + c; wb[c] = lds + WOFF + (ct >> 1) * 4096 + (8 * g + q) * 64 + ((ct & 1) ^ (g & 1)) * 32 + p * 8; }
        f32x4 acc[3] = {};
        sfor<0, 9>([&](auto I) {
            constexpr int i = I.value;
            constexpr int issued = i <= 1 ? 7 : i == 2 ? 8 : 9;
            asm volatile("s_waitcnt lgkmcnt(0)" ::: "memory");
            wait_bar<3 * (issued - 1 - i)>();
            if constexpr (i == 0) STAMP(2);
            if constexpr (i == 4) STAMP(3);
            if constexpr (i == 8) STAMP(4);
            if constexpr (i == 1 || i == 2) { if (w < 4) issue(i + 6); }
            const int kb = wrap9(kb0 + i);
#pragma unroll
            for (int s = 0; s < 2; ++s) {
                bf16x8 xf = *(LDSP bf16x8*)(xb + kb * 128 + s * 64);
#pragma unroll
                for (int c = 0; c < 3; ++c) {
                    bf16x8 wf = ld_tr(wb[c] + (i % 7) * 12288 + s * 2048, 256);
                    acc[c] = __builtin_amdgcn_mfma_f32_16x16x32_bf16(wf, xf, acc[c], 0, 0, 0);
                }
            }
        });
        u16* ho = h + (size_t)(rt * 64 + r4 * 16 + (l & 15)) * D + cb * 96 + ch * 48 + g * 4;
#pragma unroll
        for (int c = 0; c < 3; ++c)
            *(u32x2*)(ho + c * 16) = u32x2{pk2(gelu_f(acc[c][0]), gelu_f(acc[c][1])), pk2(gelu_f(acc[c][2]), gelu_f(acc[c][3]))};
        STAMP(5);
        if (cb == 0) {
            float gs = 0.f;
#pragma unroll
            for (int i = 0; i < 9; ++i) {
                const u32x4 gv = *(const u32x4*)(gw + i * 64 + (l & 7) * 8);
#pragma unroll
                for (int e = 0; e < 4; ++e) { const unsigned pr = pk2(bflo(xr[i][e]) * bflo(gv[e]), bfhi(xr[i][e]) * bfhi(gv[e])); gs += bflo(pr) + bfhi(pr); }
            }
            gs = rbf(dpp_add8(gs));
            if ((l & 7) == 0) gate[rt * 64 + w * 8 + (l >> 3)] = rbf(__builtin_amdgcn_rcpf(1.f + __builtin_amdgcn_exp2f(-1.44269504f * gs)));
        }
        DRAIN(); STAMP(6); DUMP(0, 7);
    } else {
        const int pb = xc * 8 + idx - 24, tb = pb >> 1, c0 = (pb & 1) * 288;
        const int rg = tid / 36, c = tid % 36;
        const int t0 = tb * 64 + rg * 4, chn = c0 + c * 8;
        auto rk = __builtin_amdgcn_make_buffer_rsrc((void*)k, 0, S * D * 2, 0x00020000);
        u32x4 kr[7], vr[4], sk[4];
#pragma unroll
        for (int i = 0; i < 7; ++i) kr[i] = __builtin_amdgcn_raw_buffer_load_b128(rk, (i < 3 && t0 == 0) ? 0x40000000 : ((t0 - 3 + i) * D + chn) * 2, 0, 0);
#pragma unroll
        for (int i = 0; i < 4; ++i) vr[i] = *(const u32x4*)(v + (size_t)(t0 + i) * D + chn);
#pragma unroll
        for (int i = 0; i < 4; ++i) sk[i] = *(const u32x4*)(shk + chn * 4 + i * 8);
        DRAIN(); STAMP(1);
        LDSP char* tw = lds + c * 1040 + (15 - rg) * 8;
#pragma unroll
        for (int e = 0; e < 8; ++e) {
            const float s0 = bflo(sk[e >> 1][(e & 1) * 2]), s1 = bfhi(sk[e >> 1][(e & 1) * 2]), s2 = bflo(sk[e >> 1][(e & 1) * 2 + 1]), s3 = bfhi(sk[e >> 1][(e & 1) * 2 + 1]);
            float kk[7], u[4];
#pragma unroll
            for (int i = 0; i < 7; ++i) kk[i] = (e & 1) ? bfhi(kr[i][e >> 1]) : bflo(kr[i][e >> 1]);
#pragma unroll
            for (int i = 0; i < 4; ++i) {
                const float ks = kk[i] * s3 + kk[i + 1] * s2 + kk[i + 2] * s1 + kk[i + 3] * s0;
                u[i] = ks * ((e & 1) ? bfhi(vr[i][e >> 1]) : bflo(vr[i][e >> 1]));
            }
            *(LDSP u32x2*)(tw + e * 128) = u32x2{pk2(u[3], u[2]), pk2(u[1], u[0])};
        }
        __syncthreads();
        STAMP(2);
#pragma unroll
        for (int i = 0; i < 4; ++i) {
            const int pc = tid + i * 576, cl = pc >> 3, pp = pc & 7;
            const u32x4 o = *(LDSP u32x4*)(lds + cl * 128 + (cl >> 3) * 16 + pp * 16);
            *(u32x4*)(uT + (size_t)(c0 + cl) * UP + (1984 - tb * 64) + pp * 8) = o;
        }
        if (tb == 0 && tid < 288) *(u32x4*)(uT + (size_t)(c0 + tid) * UP + 2048) = u32x4{0u, 0u, 0u, 0u};
        STAMP(3); DRAIN(); STAMP(4); STAMP(5); STAMP(6); DUMP(0, 7);
    }
}

constexpr int CV_CH = 4 * 4160 + 2 * 4576;
__global__ __launch_bounds__(512) void k_front2(const u16* __restrict__ x, const u16* __restrict__ h, const u16* __restrict__ w2,
                                                const u16* __restrict__ uT, const u16* __restrict__ ssk, const u16* __restrict__ ssD,
                                                u16* __restrict__ x1, u16* __restrict__ yT) {
    extern __shared__ __attribute__((aligned(16))) char smem[];
    LDSP char* lds = (LDSP char*)smem;
    const int tid = threadIdx.x, l = tid & 63;
    const int w = __builtin_amdgcn_readfirstlane(tid >> 6);
    const int xc = blockIdx.x & 7, idx = blockIdx.x >> 3;
    const int g = l >> 4;
    DECL; STAMP(0);
    if (idx < 12) {
        const int rt = 4 * xc + idx / 3, cb = idx % 3;
        const int kb0 = (idx / 3) * 2;
        const u32x4 rw = mk_rsrc(w2, D * D * 2), rh = mk_rsrc(h, S * D * 2);
        const unsigned vow = ((w & 3) * 16 + (l >> 2)) * 1152 + cb * 384 + (w >> 2) * 192 + ((l & 3) ^ ((l >> 5) << 1)) * 16;
        const int xrow = w * 8 + (l >> 3), xch = (l & 7) ^ ((xrow >> 1) & 7);
        auto issue = [&](int i) {
            const int kb = wrap9(kb0 + i);
            const int ck = kb * 8 + xch;
            const int sh = ck < 18 ? -1 : ck < 36 ? 1 : ck < 54 ? -2 : 2;
            const unsigned vox = (unsigned)((rt * 64 + xrow + sh) * 1152 + ck * 16);
            const unsigned slot = (i % 5) * 32768;
            dma16(rh, slot + w * 1024, vox, 0);
            const unsigned dst = slot + 8192 + (w >> 2) * 12288 + (w & 3) * 1024;
            dma16(rw, dst, vow, kb * 73728); dma16(rw, dst + 4096, vow, kb * 73728 + 64); dma16(rw, dst + 8192, vow, kb * 73728 + 128);
        };
#pragma unroll
        for (int i = 0; i < 5; ++i) issue(i);
        const int rg = w >> 2, cq = w & 3;
        const int q = (l >> 2) & 3, p = l & 3, r = l & 15;
        LDSP char* xb = lds + (rg * 32 + r) * 128;
        const int xs = (r >> 1) & 7;
        LDSP char* wb[3];
#pragma unroll
        for (int c = 0; c < 3; ++c) { const int ct = cq * 3 + c; wb[c] = lds + 8192 + (ct >> 1) * 4096 + (8 * g + q) * 64 + ((ct & 1) ^ (g & 1)) * 32 + p * 8; }
        f32x4 acc[2][3] = {};
        sfor<0, 9>([&](auto I) {
            constexpr int i = I.value;
            constexpr int issued = i == 0 ? 5 : (i + 4 > 9 ? 9 : i + 4);
            asm volatile("s_waitcnt lgkmcnt(0)" ::: "memory");
            wait_bar<4 * (issued - 1 - i)>();
            if constexpr (i == 0) STAMP(1);
            if constexpr (i == 4) STAMP(2);
            if constexpr (i == 8) STAMP(3);
            if constexpr (i >= 1 && i + 4 < 9) issue(i + 4);
#pragma unroll
            for (int s = 0; s < 2; ++s) {
                bf16x8 xf0 = *(LDSP bf16x8*)(xb + (i % 5) * 32768 + (((s * 4 + g) ^ xs) * 16));
                bf16x8 xf1 = *(LDSP bf16x8*)(xb + (i % 5) * 32768 + 2048 + (((s * 4 + g) ^ xs) * 16));
#pragma unroll
                for (int c = 0; c < 3; ++c) {
                    bf16x8 wf = ld_tr(wb[c] + (i % 5) * 32768 + s * 2048, 256);
                    acc[0][c] = __builtin_amdgcn_mfma_f32_16x16x32_bf16(wf, xf0, acc[0][c], 0, 0, 0);
                    acc[1][c] = __builtin_amdgcn_mfma_f32_16x16x32_bf16(wf, xf1, acc[1][c], 0, 0, 0);
                }
            }
        });
        STAMP(4);
        const size_t o0 = (size_t)(rt * 64 + rg * 32 + r) * D + cb * 192 + cq * 48 + g * 4;
        u32x2 xv[2][3];
#pragma unroll
        for (int t = 0; t < 2; ++t)
#pragma unroll
            for (int c = 0; c < 3; ++c) xv[t][c] = *(const u32x2*)(x + o0 + t * 16 * D + c * 16);
#pragma unroll
        for (int t = 0; t < 2; ++t)
#pragma unroll
            for (int c = 0; c < 3; ++c)
                *(u32x2*)(x1 + o0 + t * 16 * D + c * 16) = u32x2{pk2(bflo(xv[t][c][0]) + acc[t][c][0], bfhi(xv[t][c][0]) + acc[t][c][1]),
                                                               pk2(bflo(xv[t][c][1]) + acc[t][c][2], bfhi(xv[t][c][1]) + acc[t][c][3])};
        STAMP(5); DRAIN(); STAMP(6); DUMP(1, 7);
    } else {
        const int cw = w & 3, ch = (xc * 18 + idx - 12) * 4 + cw;
        const unsigned cbase = cw * CV_CH;
        const u32x4 rk = mk_rsrc(ssk + (size_t)ch * S, S * 2), ru = mk_rsrc(uT + (size_t)ch * UP, UP * 2);
        if (w < 4) {
#pragma unroll
            for (int i = 0; i < 4; ++i) {
                dma16(ru, cbase + 16640 + i * 1024, l * 16 + 2, i * 1024);
                dma16(ru, cbase + 16640 + 4576 + i * 1024, l * 16, i * 1024);
                dma16(rk, cbase + 64 + i * 1024, l * 16 + 4, i * 1024);
            }
        } else {
            const u32x4 k0 = *(const u32x4*)(ssk + (size_t)ch * S);
#pragma unroll
            for (int i = 0; i < 4; ++i)
#pragma unroll
                for (int c = 1; c < 4; ++c) dma16(rk, cbase + c * 4160 + 64 + i * 1024, l * 16 + 4 + 4 * c, i * 1024);
            if (l < 16) {
                const int c = l >> 2;
                u32x4 o = {0u, 0u, 0u, 0u};
                if ((l & 3) == 3) {
                    o[3] = c == 0 ? k0[0] : c == 1 ? k0[1] : c == 2 ? k0[2] : k0[3];
                    o[2] = c == 0 ? 0u : c == 1 ? k0[0] : c == 2 ? k0[1] : k0[2];
                    o[1] = c <= 1 ? 0u : c == 2 ? k0[0] : k0[1];
                    o[0] = c <= 2 ? 0u : k0[0];
                }
                *(LDSP u32x4*)(lds + cbase + c * 4160 + (l & 3) * 16) = o;
            }
            if (l < 56) *(LDSP u32x4*)(lds + cbase + 16640 + (l / 28) * 4576 + 4096 + (l % 28) * 16) = u32x4{0u, 0u, 0u, 0u};
        }
        const unsigned dD = ssD[ch];
        asm volatile("s_waitcnt vmcnt(0) lgkmcnt(0)\n\ts_barrier" ::: "memory");
        if (w >= 4) return;
        STAMP(1);
        const int r = l & 15;
        LDSP char* ab = lds + cbase + (r & 3) * 4160 + ((r >> 2) + g) * 16;
        LDSP char* bb = lds + cbase + 16640 + (r & 1) * 4576 + (252 - 4 * (r >> 1) + g) * 16;
        f32x4 acc[8] = {};
        bf16x8 A[2][8], B[2][8];
        auto ldf = [&](int t, bf16x8* a, bf16x8* b) {
#pragma unroll
            for (int m = 0; m < 8; ++m) { b[m] = *(LDSP bf16x8*)(bb - 3584 + (7 - m) * 512 + t * 64); a[m] = *(LDSP bf16x8*)(ab + (t + 8 * m) * 64); }
        };
        ldf(0, A[0], B[0]);
        {
            const int j = 30 - 2 * r - 8 * g;
            u32x4 dv;
#pragma unroll
            for (int e = 0; e < 4; ++e) dv[e] = (j == 2 * e) ? dD : 0u;
            const bf16x8 da = __builtin_bit_cast(bf16x8, dv);
#pragma unroll
            for (int n = 0; n < 8; ++n) acc[n] = __builtin_amdgcn_mfma_f32_16x16x32_bf16(da, B[0][n], acc[n], 0, 0, 0);
        }
#pragma unroll
        for (int t = 0; t < 8; ++t) {
            if (t < 7) ldf(t + 1, A[(t + 1) & 1], B[(t + 1) & 1]);
#pragma unroll
            for (int m = 0; m < 8; ++m)
#pragma unroll
                for (int n = 0; n + m < 8; ++n) acc[m + n] = __builtin_amdgcn_mfma_f32_16x16x32_bf16(A[t & 1][m], B[t & 1][n], acc[m + n], 0, 0, 0);
            if (t < 7) {
#pragma unroll
                for (int z = 0; z < 16; ++z) { __builtin_amdgcn_sched_group_barrier(0x8, 2, 0); __builtin_amdgcn_sched_group_barrier(0x100, 1, 0); }
            }
            SB();
        }
        STAMP(2);
        u16* yo = yT + (size_t)ch * S + 32 * (r >> 1) + 8 * g + 4 * (r & 1);
#pragma unroll
        for (int c = 0; c < 8; ++c) {
            const bool odd = r & 1;
            const float s0 = odd ? acc[c][0] : acc[c][2], s1 = odd ? acc[c][1] : acc[c][3];
            const float r0 = __builtin_bit_cast(float, __builtin_amdgcn_update_dpp(0, __builtin_bit_cast(int, s0), 0xB1, 0xf, 0xf, true));
            const float r1 = __builtin_bit_cast(float, __builtin_amdgcn_update_dpp(0, __builtin_bit_cast(int, s1), 0xB1, 0xf, 0xf, true));
            const u32x2 o = odd ? u32x2{pk2(r0, acc[c][2]), pk2(r1, acc[c][3])} : u32x2{pk2(acc[c][0], r0), pk2(acc[c][1], r1)};
            *(u32x2*)(yo + c * 256) = o;
        }
        STAMP(3); DRAIN(); STAMP(4); STAMP(5); STAMP(6); DUMP(1, 7);
    }
}

extern "C" void kernel_launch(void* const* d_in, const int* in_sizes, int n_in, void* d_out, int out_size, void* d_ws, size_t ws_size, hipStream_t stream) {
    const u16* q = (const u16*)d_in[0]; const u16* k = (const u16*)d_in[1]; const u16* v = (const u16*)d_in[2];
    const u16* shk = (const u16*)d_in[3]; const u16* ssk = (const u16*)d_in[4]; const u16* ssD = (const u16*)d_in[5];
    const u16* w1 = (const u16*)d_in[7]; const u16* w2 = (const u16*)d_in[8]; const u16* w3 = (const u16*)d_in[9]; const u16* w4 = (const u16*)d_in[10];
    const u16* gw = (const u16*)d_in[11];
    u16* ws = (u16*)((char*)d_ws + ws_size - (32u << 20));
    u16* uT = ws; float* gate = (float*)(ws + S * D + 16384); u16* yT = ws + 2 * S * D; u16* h = ws + 3 * S * D; u16* x1 = ws + 4 * S * D;
    u16* part = ws + 5 * S * D;
    (void)hipFuncSetAttribute((const void*)k_front1, hipFuncAttributeMaxDynamicSharedMemorySize, 163840);
    (void)hipFuncSetAttribute((const void*)k_front2, hipFuncAttributeMaxDynamicSharedMemorySize, 163840);
    (void)hipFuncSetAttribute((const void*)k_mlp, hipFuncAttributeMaxDynamicSharedMemorySize, 163840);
    k_front1<<<256, 576, 163840, stream>>>(q, w1, gw, k, v, shk, h, gate, uT);
    k_front2<<<240, 512, 163840, stream>>>(q, h, w2, uT, ssk, ssD, x1, yT);
    k_mlp<<<256, 512, 163840, stream>>>(x1, w3, w4, part);
    k_fin<<<576, 256, 0, stream>>>(part, x1, yT, q, gate, (u16*)d_out);
}
```

```cpp
#include <hip/hip_runtime.h>
#include <hip/hip_bf16.h>
#include <stdint.h>
#include <type_traits>

typedef unsigned short u16;
typedef __attribute__((ext_vector_type(4))) short s16x4;
typedef __attribute__((ext_vector_type(8))) __bf16 bf16x8;
typedef __attribute__((ext_vector_type(2))) __bf16 bf16x2;
typedef __attribute__((ext_vector_type(4))) float f32x4;
typedef __attribute__((ext_vector_type(2))) float f32x2;
typedef __attribute__((ext_vector_type(4))) unsigned u32x4;
typedef __attribute__((ext_vector_type(2))) unsigned u32x2;
#define LDSP __attribute__((address_space(3)))
#define DI __device__ __forceinline__
#define SB() __builtin_amdgcn_sched_barrier(0)
#define IC(v) std::integral_constant<int, (v)>{}

constexpr int S = 2048, D = 576, F = 4608;
constexpr float GS = 0.46866869f;
constexpr float GALPHA = -4.91222954f;
constexpr float GINV = 2.13370343f;

template <int I, int N, class Fn> DI void sfor(Fn&& f) {
    if constexpr (I < N) { f(std::integral_constant<int, I>{}); sfor<I + 1, N>(f); }
}
DI float bflo(unsigned v) { return __uint_as_float(v << 16); }
DI float bfhi(unsigned v) { return __uint_as_float(v & 0xffff0000u); }
DI unsigned pk2(float a, float b) { f32x2 v = {a, b}; return __builtin_bit_cast(unsigned, __builtin_convertvector(v, bf16x2)); }
DI float rbf(float f) { return bflo(pk2(f, 0.f)); }
DI int wrap9(int v) { return v >= 9 ? v - 9 : v; }
DI u32x4 mk_rsrc(const void* p, unsigned bytes) {
    unsigned long long a = (unsigned long long)p;
    u32x4 r = {(unsigned)a, (unsigned)(a >> 32) & 0xffffu, bytes, 0x00020000u};
    return r;
}
DI void dma16(u32x4 rs, unsigned lds, unsigned voff, unsigned soff) {
    asm volatile("s_mov_b32 m0, %0\n\ts_nop 0\n\tbuffer_load_dwordx4 %1, %2, %3 offen lds" :: "s"(lds), "v"(voff), "s"(rs), "s"(soff) : "memory");
}
DI void dma16c(u32x4 rs, unsigned lds, unsigned voff, unsigned soff) {
    asm volatile("s_mov_b32 m0, %0\n\ts_nop 0\n\tbuffer_load_dwordx4 %1, %2, %3 offen sc1 lds" :: "s"(lds), "v"(voff), "s"(rs), "s"(soff) : "memory");
}
DI void dma16n(u32x4 rs, unsigned lds, unsigned voff, unsigned soff) {
    asm volatile("s_mov_b32 m0, %0\n\ts_nop 0\n\tbuffer_load_dwordx4 %1, %2, %3 offen nt lds" :: "s"(lds), "v"(voff), "s"(rs), "s"(soff) : "memory");
}
DI void ld16(u32x4& d, u32x4 rs, unsigned vo, unsigned so) { asm volatile("buffer_load_dwordx4 %0, %1, %2, %3 offen" : "=v"(d) : "v"(vo), "s"(rs), "s"(so)); }
template <int OFF> DI void ld4(unsigned& d, u32x4 rs, unsigned vo) { asm volatile("buffer_load_dword %0, %1, %2, 0 offen offset:%3" : "=v"(d) : "v"(vo), "s"(rs), "n"(OFF)); }
DI void ld16c(u32x4& d, u32x4 rs, unsigned vo, unsigned so) { asm volatile("buffer_load_dwordx4 %0, %1, %2, %3 offen sc1" : "=v"(d) : "v"(vo), "s"(rs), "s"(so)); }
DI void ld4c(unsigned& d, u32x4 rs, unsigned vo, unsigned so) { asm volatile("buffer_load_dword %0, %1, %2, %3 offen sc1" : "=v"(d) : "v"(vo), "s"(rs), "s"(so)); }
DI void st16c(u32x4 v, u32x4 rs, unsigned vo, unsigned so) { asm volatile("buffer_store_dwordx4 %0, %1, %2, %3 offen sc1\n\ts_nop 1" :: "v"(v), "v"(vo), "s"(rs), "s"(so) : "memory"); }
DI void st4c(unsigned v, u32x4 rs, unsigned vo, unsigned so) { asm volatile("buffer_store_dword %0, %1, %2, %3 offen sc1" :: "v"(v), "v"(vo), "s"(rs), "s"(so) : "memory"); }
template <int N> DI void wait_vm() { asm volatile("s_waitcnt vmcnt(%0)" :: "n"(N) : "memory"); }
DI void bar() { asm volatile("s_barrier" ::: "memory"); }
#define OAUX 0
#define FILLV 1
#define XORD 1
#define LA1 3
#define W2PACE 4
#define WSLEEP 32
#define W3A 72
#define WAUX 0
#define FAUX 16
DI void stf(void* base, unsigned bytes, unsigned vo, u32x4 v) {
    __builtin_amdgcn_raw_buffer_store_b128(v, __builtin_amdgcn_make_buffer_rsrc(base, 0, bytes, 0x00020000), vo, 0, FAUX);
}
template <int N> DI void wait_bar() { asm volatile("s_waitcnt vmcnt(%0)\n\ts_barrier" :: "n"(N) : "memory"); }
template <int N> DI void wait_bar_l() { asm volatile("s_waitcnt vmcnt(%0) lgkmcnt(0)\n\ts_barrier" :: "n"(N) : "memory"); }
DI void lds_bar() { asm volatile("s_waitcnt lgkmcnt(0)\n\ts_barrier" ::: "memory"); }
DI bf16x8 ld_tr(LDSP char* a, int second) {
    s16x4 lo = __builtin_amdgcn_ds_read_tr16_b64_v4i16((LDSP s16x4*)a);
    s16x4 hi = __builtin_amdgcn_ds_read_tr16_b64_v4i16((LDSP s16x4*)(a + second));
    return __builtin_bit_cast(bf16x8, __builtin_shufflevector(lo, hi, 0, 1, 2, 3, 4, 5, 6, 7));
}
DI LDSP char* opaque(LDSP char* p) { asm volatile("" : "+v"(p)); return p; }
DI f32x4 mfma(bf16x8 a, bf16x8 b, f32x4 c) { return __builtin_amdgcn_mfma_f32_16x16x32_bf16(a, b, c, 0, 0, 0); }
DI float sum_g(float v) {
    v += __shfl_xor(v, 16); v += __shfl_xor(v, 32); return v;
}
DI float gelu_f(float x) {
    const float u = x * GS;
    const float e = __builtin_amdgcn_exp2f(u * __builtin_fmaf(-u, u, GALPHA));
    return x * __builtin_amdgcn_rcpf(1.f + e);
}
template <bool FIRST> DI void mfma2(f32x4& c0, f32x4& c1, const bf16x8& a, const bf16x8& b0, const bf16x8& b1) {
    if constexpr (FIRST) asm volatile("v_mfma_f32_16x16x32_bf16 %0, %2, %3, 0\n\tv_mfma_f32_16x16x32_bf16 %1, %2, %4, 0" : "=&v"(c0), "=&v"(c1) : "v"(a), "v"(b0), "v"(b1));
    else asm volatile("v_mfma_f32_16x16x32_bf16 %0, %2, %3, %0\n\tv_mfma_f32_16x16x32_bf16 %1, %2, %4, %1" : "+v"(c0), "+v"(c1) : "v"(a), "v"(b0), "v"(b1));
}
template <bool FIRST> DI void mfma1(f32x4& c0, const bf16x8& a, const bf16x8& b0) {
    if constexpr (FIRST) asm volatile("v_mfma_f32_16x16x32_bf16 %0, %1, %2, 0" : "=&v"(c0) : "v"(a), "v"(b0));
    else asm volatile("v_mfma_f32_16x16x32_bf16 %0, %1, %2, %0" : "+v"(c0) : "v"(a), "v"(b0));
}
#define STAMP(i) do {} while (0)
#define DUMP(kid, n) do {} while (0)
#define DECL do {} while (0)
#define DRAIN() do {} while (0)
struct GeluT { float t0, t1, t2; };
template <int e> DI void gelu_even(GeluT& T, const f32x4* P) {
    if constexpr (e == 0) asm volatile("v_fma_f32 %0, -%1, %1, %2" : "=v"(T.t0) : "v"(P[0][0]), "s"(GALPHA));
    else if constexpr (e < 8) asm volatile("v_add_f32 %1, 1.0, %0\n\tv_fma_f32 %0, -%2, %2, %3\n\tv_rcp_f32 %1, %1" : "+v"(T.t0), "=&v"(T.t1) : "v"(P[e >> 2][e & 3]), "s"(GALPHA));
    else asm volatile("v_add_f32 %0, 1.0, %1\n\ts_nop 0\n\tv_rcp_f32 %0, %0" : "=&v"(T.t1) : "v"(T.t0));
}
template <int e> DI void gelu_odd(GeluT& T, const f32x4* P, unsigned* Go) {
    if constexpr (e == 0) asm volatile("v_mul_f32 %0, %0, %1\n\ts_nop 0\n\tv_exp_f32 %0, %0" : "+v"(T.t0) : "v"(P[0][0]));
    else if constexpr (e < 8 && (e & 1)) asm volatile("v_mul_f32 %0, %0, %3\n\tv_mul_f32 %1, %2, %4\n\tv_exp_f32 %0, %0" : "+v"(T.t0), "=&v"(T.t2) : "v"(T.t1), "v"(P[e >> 2][e & 3]), "v"(P[(e - 1) >> 2][(e - 1) & 3]));
    else if constexpr (e < 8) asm volatile("v_mul_f32 %0, %0, %4\n\tv_mul_f32 %1, %1, %5\n\tv_exp_f32 %0, %0\n\tv_cvt_pk_bf16_f32 %2, %3, %1" : "+v"(T.t0), "+v"(T.t1), "=&v"(Go[(e - 1) >> 1]) : "v"(T.t2), "v"(P[e >> 2][e & 3]), "v"(P[(e - 1) >> 2][(e - 1) & 3]));
    else asm volatile("v_mul_f32 %0, %0, %3\n\ts_nop 0\n\tv_cvt_pk_bf16_f32 %1, %2, %0" : "+v"(T.t1), "=&v"(Go[3]) : "v"(T.t2), "v"(P[1][3]));
}

constexpr int HALF = 73728;
#define MAXPG1 6
#define MAXPG2 6
#define PRE 4
#define ARR1 2
#define ARR2 0
struct Sched { int dma[54][6]; int vm[54]; int pollwait, ywait; };
constexpr int avail(int n) { return n < 9 ? 0 : n < 18 ? 1 + (n - 9) / 3 : (n >= 90 && n < 99) ? 28 + (n - 90) / 3 : (n - 18) / 3 + 1; }
constexpr int G_PUB = 29, G_POLL = 33, G_CHK = 37, G_YPOLL = 20, G_YCHK = 26;
constexpr int n_store(int G, int k) { return (G >= 30 && G % 3 == 0 && k == 1) ? 1 : 0; }
constexpr int fill_at(int G, int k) {
    return (k == 1 && G >= 7 && G <= 21 && (G & 1)) ? (G - 7) / 2 : -1;
}
constexpr int get_at(int G, int k) {
    return (G == 53 && k == 3) ? 7 : k != 3 ? -1 : G == 52 ? 6 : (G >= 38 && G <= 50 && (G - 38) % 3 == 0) ? 1 + (G - 38) / 3 : -1;
}
constexpr int n_other(int G, int k) { return (fill_at(G, k) >= 0) + (get_at(G, k) >= 0) + ((G == 27 && k < 5) ? 1 : 0); }
constexpr Sched make_sched() {
    Sched s{};
    int next = 9;
    for (int G = 0; G < 54; ++G) {
        int cnt = 0; const int mx = G < 27 ? MAXPG1 : MAXPG2;
        while (next + cnt < 162 && avail(next + cnt) <= G && cnt < mx) ++cnt;
        for (int k = 0; k < 6; ++k) s.dma[G][k] = -1;
        for (int c = 0; c < cnt; ++c) s.dma[G][G == 1 ? 2 + c : cnt <= 3 ? 2 * c : c] = next + c;
        next += cnt;
    }
    int pos_dma[162] = {}, before[55] = {};
    int p = 0;
    for (int n = 0; n < 9; ++n) pos_dma[n] = p++;
    p += 12;
    for (int G = 0; G < 54; ++G) {
        before[G] = p;
        for (int k = 0; k < 6; ++k) {
            if (s.dma[G][k] >= 0) pos_dma[s.dma[G][k]] = p++;
            p += n_store(G, k) + n_other(G, k);
        }
    }
    for (int G = 0; G < 54; ++G) {
        int need = 3 * G + 3; if (need > 161 || G == 51) need = 161;
        int last = pos_dma[need];
        if (G == G_PUB && before[27] - 1 > last) last = before[27] - 1;
        if (G == 51 && before[48] - 1 > last) last = before[48] - 1;
        s.vm[G] = last < before[G] ? before[G] - 1 - last : -1;
    }
    s.pollwait = before[G_CHK] - before[G_POLL];
    s.ywait = before[G_YCHK] - before[G_YPOLL];
    return s;
}
constexpr Sched SCH = make_sched();

DI unsigned norm2(unsigned v, float sc, float of) {
    float a, b; unsigned o;
    asm volatile("v_lshlrev_b32 %0, 16, %3\n\tv_and_b32 %1, 0xffff0000, %3\n\tv_fma_f32 %0, %0, %4, %5\n\tv_fma_f32 %1, %1, %4, %5\n\ts_nop 0\n\tv_cvt_pk_bf16_f32 %2, %0, %1"
                 : "=&v"(a), "=&v"(b), "=v"(o) : "v"(v), "v"(sc), "v"(of));
    return o;
}
DI unsigned gelu2(float u0, float u1) {
    float a, b; unsigned o;
    asm volatile("v_fma_f32 %0, -%3, %3, %5\n\tv_fma_f32 %1, -%4, %4, %5\n\tv_mul_f32 %0, %0, %3\n\tv_mul_f32 %1, %1, %4\n\tv_exp_f32 %0, %0\n\tv_exp_f32 %1, %1\n\t"
                 "v_add_f32 %0, 1.0, %0\n\tv_add_f32 %1, 1.0, %1\n\tv_rcp_f32 %0, %0\n\tv_rcp_f32 %1, %1\n\tv_mul_f32 %0, %0, %3\n\tv_mul_f32 %1, %1, %4\n\ts_nop 0\n\t"
                 "v_cvt_pk_bf16_f32 %2, %0, %1" : "=&v"(a), "=&v"(b), "=v"(o) : "v"(u0), "v"(u1), "s"(GALPHA));
    return o;
}
DI void touch(int skip, u32x4 rs, unsigned vo, unsigned& sink) {
    asm volatile("s_cmp_lg_u32 %1, 0\n\ts_cbranch_scc1 1f\n\tbuffer_load_dword %0, %3, %2, 0 offen\n\t1:" : "+v"(sink) : "s"(skip), "s"(rs), "v"(vo) : "scc");
}
struct Side { u32x4 x, gq, y; };
DI void mix_base(float& b0, float& b1, unsigned qq, unsigned y, float gv) {
    float t0, t1;
    asm volatile("v_lshlrev_b32 %0, 16, %4\n\tv_and_b32 %1, 0xffff0000, %4\n\tv_lshlrev_b32 %2, 16, %5\n\tv_and_b32 %3, 0xffff0000, %5\n\t"
                 "v_mul_f32 %0, %0, %2\n\tv_mul_f32 %1, %1, %3\n\tv_mul_f32 %0, %0, %6\n\tv_mul_f32 %1, %1, %6"
                 : "=&v"(b0), "=&v"(b1), "=&v"(t0), "=&v"(t1) : "v"(qq), "v"(y), "v"(gv));
}
DI u32x4 mix_out(const float* base, const float* ngs, const f32x4* sum, LDSP char* scr, int l) {
#pragma unroll
    for (int t = 0; t < 2; ++t)
        *(LDSP u32x2*)(scr + (t * 16 + (l & 15)) * 48 + (l >> 4) * 8) =
            u32x2{pk2(base[4 * t] + ngs[t] * sum[t][0], base[4 * t + 1] + ngs[t] * sum[t][1]), pk2(base[4 * t + 2] + ngs[t] * sum[t][2], base[4 * t + 3] + ngs[t] * sum[t][3])};
    asm volatile("s_waitcnt lgkmcnt(0)" ::: "memory");
    return *(LDSP u32x4*)(scr + (l >> 1) * 48 + (l & 1) * 16);
}
DI void add_piece(f32x4& c0, f32x4& c1, const u32x4& s0, const u32x4& s1, const u32x4& v) {
    asm volatile("v_mfma_f32_16x16x32_bf16 %0, %2, %4, %0\n\tv_mfma_f32_16x16x32_bf16 %1, %3, %4, %1" : "+v"(c0), "+v"(c1) : "v"(s0), "v"(s1), "v"(v));
}
DI unsigned umax4(const u32x4& v) { return max(max(v[0], v[1]), max(v[2], v[3])); }
template <int N> DI void settle(u32x4* v, const unsigned* so, u32x4 rp, unsigned vo) {
#pragma unroll 1
    for (int it = 0; it < (1 << 20); ++it) {
        unsigned m = 0;
#pragma unroll
        for (int i = 0; i < N; ++i) m = max(m, umax4(v[i]));
        if (__builtin_amdgcn_ballot_w64(m == 0xffffffffu) == 0) break;
        if constexpr (N == 3)
            asm volatile("buffer_load_dwordx4 %0, %3, %4, %5 offen sc1\n\tbuffer_load_dwordx4 %1, %3, %4, %6 offen sc1\n\t"
                         "buffer_load_dwordx4 %2, %3, %4, %7 offen sc1\n\ts_waitcnt vmcnt(0)"
                         : "=&v"(v[0]), "=&v"(v[1]), "=&v"(v[2]) : "v"(vo), "s"(rp), "s"(so[0]), "s"(so[1]), "s"(so[2]) : "memory");
        else
            asm volatile("buffer_load_dwordx4 %0, %4, %5, %6 offen sc1\n\tbuffer_load_dwordx4 %1, %4, %5, %7 offen sc1\n\t"
                         "buffer_load_dwordx4 %2, %4, %5, %8 offen sc1\n\tbuffer_load_dwordx4 %3, %4, %5, %9 offen sc1\n\ts_waitcnt vmcnt(0)"
                         : "=&v"(v[0]), "=&v"(v[1]), "=&v"(v[2]), "=&v"(v[3]) : "v"(vo), "s"(rp), "s"(so[0]), "s"(so[1]), "s"(so[2]), "s"(so[3]) : "memory");
    }
    asm volatile("s_nop 1");
}
DI void ld_shared(u32x4* ev, Side& se, int skip, u32x4 rs, unsigned vo, unsigned so, unsigned po) {
    unsigned t;
    asm volatile("s_cmp_lg_u32 %12, 0\n\ts_cbranch_scc1 1f\n\t"
                 "buffer_load_dwordx4 %0, %14, %13, %15 offen sc1\n\ts_add_u32 %11, %15, 0x2000\n\t"
                 "buffer_load_dwordx4 %1, %14, %13, %11 offen sc1\n\ts_add_u32 %11, %15, 0x4000\n\t"
                 "buffer_load_dwordx4 %2, %14, %13, %11 offen sc1\n\ts_add_u32 %11, %15, 0x6000\n\t"
                 "buffer_load_dwordx4 %3, %14, %13, %11 offen sc1\n\ts_add_u32 %11, %15, 0x8000\n\t"
                 "buffer_load_dwordx4 %4, %14, %13, %11 offen sc1\n\ts_add_u32 %11, %15, 0xa000\n\t"
                 "buffer_load_dwordx4 %5, %14, %13, %11 offen sc1\n\ts_add_u32 %11, %15, 0xc000\n\t"
                 "buffer_load_dwordx4 %6, %14, %13, %11 offen sc1\n\ts_add_u32 %11, %15, 0xe000\n\t"
                 "buffer_load_dwordx4 %7, %14, %13, %11 offen sc1\n\t"
                 "buffer_load_dwordx4 %8, %14, %13, %16 offen sc1\n\ts_add_u32 %11, %16, %17\n\t"
                 "buffer_load_dwordx4 %9, %14, %13, %11 offen sc1\n\ts_add_u32 %11, %11, %17\n\t"
                 "buffer_load_dwordx4 %10, %14, %13, %11 offen sc1\n\t"
                 "1:"
                 : "=&v"(ev[0]), "=&v"(ev[1]), "=&v"(ev[2]), "=&v"(ev[3]), "=&v"(ev[4]), "=&v"(ev[5]), "=&v"(ev[6]), "=&v"(ev[7]),
                   "=&v"(se.x), "=&v"(se.gq), "=&v"(se.y), "=&s"(t)
                 : "s"(skip), "s"(rs), "v"(vo), "s"(so), "s"(po), "n"(S * D * 2) : "scc");
}
DI void ld_flag(u32x4& v, int skip, u32x4 rs, unsigned vf) {
    asm volatile("s_cmp_lg_u32 %1, 0\n\ts_cbranch_scc1 1f\n\tbuffer_load_dwordx4 %0, %3, %2, 0 offen sc1\n\t1:" : "=&v"(v) : "s"(skip), "s"(rs), "v"(vf) : "scc");
}
DI void await(u32x4 v, unsigned tag, u32x4 rs, unsigned vf) {
#pragma unroll 1
    for (int it = 0; it < (1 << 20); ++it) {
        if (__builtin_amdgcn_ballot_w64(((v[0] ^ tag) | (v[1] ^ tag) | (v[2] ^ tag) | (v[3] ^ tag)) != 0u) == 0) break;
        __builtin_amdgcn_s_sleep(2);
        asm volatile("buffer_load_dwordx4 %0, %1, %2, 0 offen sc1\n\ts_waitcnt vmcnt(0)" : "=v"(v) : "v"(vf), "s"(rs) : "memory");
    }
}

DI void sld3(unsigned& a, u32x4& b4, unsigned& c, const void* pa, const void* pb, const void* pc) {
    asm volatile("s_load_dword %0, %3, 0x0\n\ts_load_dwordx4 %1, %4, 0x0\n\ts_load_dword %2, %5, 0x0\n\ts_waitcnt lgkmcnt(0)"
                 : "=&s"(a), "=&s"(b4), "=&s"(c) : "s"(pa), "s"(pb), "s"(pc) : "memory");
}

constexpr unsigned NB = S * D * 2;
constexpr unsigned O_FA = 4096, O_FB = O_FA + 32768, O_FP = O_FB + 32768, O_GATE = O_FP + 32768, O_STAT = O_GATE + 8192;
constexpr unsigned O_X1 = 524288, O_Q = O_X1 + NB, O_Y = O_Q + NB, O_H = O_Y + NB, O_U = O_H + NB, O_PART = O_U + NB, WS_END = O_PART + 8 * NB;
constexpr int SL = 24576, HRING = 5 * SL, AUXO = HRING + 32768;
constexpr int CV_U = 4 * 16640, CV_Y = CV_U + 4 * 9152;
constexpr int g2_after(int i) {
    int pos_h[9] = {}, pos_w[9] = {}, p = 0, before = 0;
    for (int j = 0; j < 4; ++j) pos_w[j] = -1;
    for (int j = 0; j < 3; ++j) { p += 2; pos_h[j] = p - 1; }
    p += 6; pos_w[4] = p - 1;
    for (int j = 0; j <= i; ++j) {
        before = p;
        if (j + 3 < 9) { p += 2; pos_h[j + 3] = p - 1; }
        if (j >= 1 && j + 4 < 9) { p += 6; pos_w[j + 4] = p - 1; }
    }
    const int last = pos_h[i] > pos_w[i] ? pos_h[i] : pos_w[i];
    return before - 1 - last;
}
typedef __attribute__((ext_vector_type(4))) __bf16 bf16x4;
DI f32x4 mfma_sq(unsigned a, unsigned b, f32x4 c) {
    const s16x4 v = __builtin_bit_cast(s16x4, u32x2{a, b});
    return __builtin_amdgcn_mfma_f32_16x16x16bf16_1k(v, v, c, 0, 0, 0);
}
DI float pick4(const f32x4& v, int i) { return i == 0 ? v[0] : i == 1 ? v[1] : i == 2 ? v[2] : v[3]; }
DI void warm(const void* base, unsigned bytes, int first, int step, int end, int pitch, int per, int col) {
    auto rs = __builtin_amdgcn_make_buffer_rsrc((void*)base, 0, bytes, 0x00020000);
    unsigned a = 0;
#pragma unroll 1
    for (int ln = first; ln < end; ln += step) a |= __builtin_amdgcn_raw_buffer_load_b32(rs, (ln / per) * pitch + col + (ln % per) * 128, 0, 0);
    asm volatile("" :: "v"(a));
}

__global__ __launch_bounds__(512, 2) void k_all(const u16* __restrict__ x, const u16* __restrict__ kin, const u16* __restrict__ vin, const u16* __restrict__ shk,
                                                const u16* __restrict__ ssk, const u16* __restrict__ ssD, const u16* __restrict__ w1, const u16* __restrict__ w2,
                                                const u16* __restrict__ w3, const u16* __restrict__ w4, const u16* __restrict__ gwt,
                                                char* __restrict__ ws, u16* __restrict__ out) {
    extern __shared__ __attribute__((aligned(16))) char smem[];
    LDSP char* lds = (LDSP char*)smem;
    const int tid = threadIdx.x, l = tid & 63;
    const int w = __builtin_amdgcn_readfirstlane(tid >> 6);
    const int b = blockIdx.x, xc = b & 7, idx = b >> 3;
    const int g = l >> 4, q = (l >> 2) & 3, p = l & 3, r = l & 15;
    const unsigned vl16 = l * 16;
    asm volatile("" : "+s"(x), "+s"(kin), "+s"(vin), "+s"(shk), "+s"(ssk), "+s"(ssD), "+s"(w1), "+s"(w2), "+s"(w3), "+s"(w4), "+s"(gwt), "+s"(ws), "+s"(out));
    const u32x4 rws = mk_rsrc(ws, WS_END);
    unsigned tag;
    const int sw = ((g & 1) << 1) | (q >> 1);
    const int cg = xc * 18 + idx - 14, cw = w & 3, ch = cg * 4 + cw;
    const u32x4 rk = mk_rsrc(ssk + (size_t)(idx >= 14 ? ch : 0) * S, S * 2);
    u32x4 k0; unsigned dD;
    auto scalars = [&]() {
        const int c2 = idx >= 14 ? ch : 0;
        sld3(tag, k0, dD, ws, ssk + (size_t)c2 * S, ssD + (c2 & ~1));
        tag = tag * 1664525u + 1013904223u;
        dD = (c2 & 1) ? dD >> 16 : dD & 0xffffu;
    };
    auto conv_k1 = [&]() {
#pragma unroll
        for (int i = 0; i < 4; ++i) dma16n(rk, cw * 16640 + 64 + i * 1024, vl16 + 4, i * 1024);
    };
    { DECL; STAMP(0);
    if (idx < 18) {
        const int rt = 2 * xc + idx / 9, cb = idx % 9;
        const int kb0 = wrap9(cb + (idx / 9) * 4);
        if (w >= 4) {
            const int u = w - 4;
            const u32x4 rx = mk_rsrc(x + (size_t)rt * 128 * D, 128 * D * 2), rw = mk_rsrc(w1, D * D * 2), rgw = mk_rsrc(gwt, D * 2), rw2 = mk_rsrc(w2, D * D * 2);
            const int lrow = u * 16 + (l >> 3);
            const unsigned vx0 = lrow * 1152 + (((l & 7) ^ g) * 16), vx1 = (lrow + 8) * 1152 + (((l & 7) ^ (4 + g)) * 16);
            const unsigned vw0 = lrow * 1152 + ((l & 7) ^ ((g & 1) << 1)) * 16, vw1 = (lrow + 8) * 1152 + ((l & 7) ^ (((g & 1) | 2) << 1)) * 16;
            auto issue = [&](int i) {
                const int kb = wrap9(kb0 + i);
                const unsigned dst = (i % 5) * SL + u * 2048;
                dma16(rx, dst, vx0, kb * 128); dma16(rx, dst + 1024, vx1, kb * 128);
                dma16(rw, dst + 16384, vw0, kb * 73728 + cb * 128); dma16(rw, dst + 16384 + 1024, vw1, kb * 73728 + cb * 128);
                dma16(rx, dst + 8192, vx0, kb * 128 + 64 * 1152); dma16(rx, dst + 8192 + 1024, vx1, kb * 128 + 64 * 1152);
            };
            dma16(rgw, AUXO + (u & 1) * 1024, vl16, (u & 1) * 1024);
            issue(0); issue(1);
            sfor<0, 9>([&](auto I) {
                constexpr int i = I.value;
                wait_bar<(i == 0 ? 6 : i < 7 ? 6 * (LA1 - 1) : 6 * (8 - i))>();
                if constexpr (i == 0 && LA1 == 3) issue(2);
                if constexpr (i + LA1 < 9) issue(i + LA1);
            });
            if (idx < 12) {
                const int kb2 = wrap9((idx % 3) * 3 + (idx / 3) * 2);
                const u32x4 rr = mk_rsrc(x + (size_t)(4 * xc + idx / 3) * 64 * D, 64 * D * 2);
#pragma unroll
                for (int c = 0; c < 3; ++c) { dma16(rr, 4 * SL + u * 2048 + c * 8192, vx0, (idx % 3) * 384 + c * 128); dma16(rr, 4 * SL + u * 2048 + 1024 + c * 8192, vx1, (idx % 3) * 384 + c * 128); }
#pragma unroll
                for (int j = 0; j < 4; ++j) {
                    const unsigned dst = j * SL + u * 2048, so = wrap9(kb2 + j) * 73728 + (idx % 3) * 384;
#pragma unroll
                    for (int c = 0; c < 3; ++c) { dma16(rw2, dst + c * 8192, vw0, so + c * 128); dma16(rw2, dst + 1024 + c * 8192, vw1, so + c * 128); }
                    if (j == 0) wait_bar<6>();
                    wait_vm<W2PACE>();
                }
            } else {
                bar();
                if (idx >= 14) conv_k1();
            }
            scalars();
        } else {
            scalars();
            if (tid < 4) *(LDSP unsigned*)(lds + AUXO + 2048 + tid * 4) = 0x3f803f80u;
            LDSP char* xb[2][2]; LDSP char* wb[2][4];
#pragma unroll
            for (int hi = 0; hi < 2; ++hi) {
#pragma unroll
                for (int s2 = 0; s2 < 2; ++s2) xb[hi][s2] = opaque(lds + hi * 2 * SL + (w * 32 + r) * 128 + (((4 * s2 + g) ^ (r >> 1)) * 16));
#pragma unroll
                for (int c = 0; c < 4; ++c) wb[hi][c] = opaque(lds + hi * 2 * SL + 16384 + (8 * g + q) * 128 + (((c + w) & 3) ^ sw) * 32 + p * 8);
            }
            LDSP char* ob = lds + AUXO + (r == 1 ? g * 16 : 2048);
            const int omul = r == 1 ? 64 : 0;
            u32x4 onesu = {0x3f803f80u, 0x3f803f80u, 0x3f803f80u, 0x3f803f80u};
            asm volatile("" : "+v"(onesu));
            const bf16x8 ones = __builtin_bit_cast(bf16x8, onesu);
            f32x4 acc[2][4] = {}, a_sum[2] = {}, a_sq[2] = {}, a_cs = {};
            bf16x8 xf[2][2], wf[2][4], of[2];
            sfor<0, 19>([&](auto KS) {
                constexpr int n = KS.value, i = n >> 1, s2 = n & 1, nb = n & 1, a = nb ^ 1;
                constexpr int sl = i % 5, hi = sl >= 2, off = (sl - 2 * hi) * SL;
                if constexpr (s2 == 0 && n < 18) {
                    lds_bar();
                    if constexpr (i == 0) STAMP(1);
                    if constexpr (i == 8) STAMP(2);
                }
                auto rd = [&](int e) {
                    if constexpr (n < 18) {
                        if (e < 2) xf[nb][e] = *(LDSP bf16x8*)(xb[hi][s2] + off + e * 2048);
                        else if (e < 6) wf[nb][e - 2] = ld_tr(wb[hi][e - 2] + off + s2 * 4096, 512);
                        else if (e == 6) of[nb] = *(LDSP bf16x8*)(ob + omul * (wrap9(kb0 + i) * 2 + s2));
                    }
                    SB();
                };
#pragma unroll
                for (int m = 0; m < 13; ++m) {
                    if constexpr (n >= 1) {
                        if (m < 2) a_sq[m] = mfma(xf[a][m], xf[a][m], a_sq[m]);
                        else if (m == 2) a_cs = mfma(wf[a][0], ones, a_cs);
                        else if (m < 11) acc[(m - 3) & 1][(m - 3) >> 1] = mfma(wf[a][(m - 3) >> 1], xf[a][(m - 3) & 1], acc[(m - 3) & 1][(m - 3) >> 1]);
                        else a_sum[m - 11] = mfma(of[a], xf[a][m - 11], a_sum[m - 11]);
                        SB();
                    }
                    if (m < 7) rd(m);
                }
            });
            STAMP(3);
            LDSP float* st = (LDSP float*)(lds + AUXO + 4096);
            if (r == 0) *(LDSP f32x4*)(st + w * 16 + g * 4) = a_cs;
            const float sq0 = __builtin_bit_cast(float, __builtin_amdgcn_ds_bpermute(((r >> 2) * 16 + r) * 4, __builtin_bit_cast(int, pick4(a_sq[0], r & 3))));
            const float sq1 = __builtin_bit_cast(float, __builtin_amdgcn_ds_bpermute(((r >> 2) * 16 + r) * 4, __builtin_bit_cast(int, pick4(a_sq[1], r & 3))));
            float gd = __builtin_bit_cast(float, __builtin_amdgcn_ds_bpermute(r * 4, __float_as_int(a_sum[1][1])));
            lds_bar();
            asm volatile("" : "+v"(gd));
            f32x4 cs[4];
#pragma unroll
            for (int c = 0; c < 4; ++c) cs[c] = *(LDSP f32x4*)(st + ((c + w) & 3) * 16 + g * 4);
            float mr[2], rs[2];
#pragma unroll
            for (int t = 0; t < 2; ++t) {
                const float mean = a_sum[t][0] * (1.f / D);
                rs[t] = GS * __builtin_amdgcn_rsqf(fmaxf((t ? sq1 : sq0) * (1.f / D) - mean * mean, 0.f) + 1e-5f);
                mr[t] = -mean * rs[t];
            }
            LDSP char* hw = lds + HRING + w * 32 * 144;
            const f32x2 al = {GALPHA, GALPHA};
            SB();
#pragma unroll
            for (int t = 0; t < 2; ++t) {
                f32x2 u[8], z[8];
#pragma unroll
                for (int e = 0; e < 8; ++e) u[e] = f32x2{cs[e >> 1][(e & 1) * 2], cs[e >> 1][(e & 1) * 2 + 1]} * mr[t];
                SB();
#pragma unroll
                for (int e = 0; e < 8; ++e) u[e] = __builtin_elementwise_fma(f32x2{acc[t][e >> 1][(e & 1) * 2], acc[t][e >> 1][(e & 1) * 2 + 1]}, f32x2{rs[t], rs[t]}, u[e]);
                SB();
#pragma unroll
                for (int e = 0; e < 8; ++e) z[e] = __builtin_elementwise_fma(-u[e], u[e], al);
                SB();
#pragma unroll
                for (int e = 0; e < 8; ++e) z[e] *= u[e];
                SB();
#pragma unroll
                for (int e = 0; e < 8; ++e) z[e] = f32x2{__builtin_amdgcn_exp2f(z[e][0]), __builtin_amdgcn_exp2f(z[e][1])};
                SB();
#pragma unroll
                for (int e = 0; e < 8; ++e) z[e] += 1.f;
                SB();
#pragma unroll
                for (int e = 0; e < 8; ++e) z[e] = f32x2{__builtin_amdgcn_rcpf(z[e][0]), __builtin_amdgcn_rcpf(z[e][1])};
                SB();
#pragma unroll
                for (int e = 0; e < 8; ++e) z[e] *= u[e];
                SB();
#pragma unroll
                for (int c = 0; c < 4; ++c)
                    *(LDSP u32x2*)(hw + (t * 16 + r) * 144 + ((c + w) & 3) * 32 + g * 8) = u32x2{pk2(z[2 * c][0], z[2 * c][1]), pk2(z[2 * c + 1][0], z[2 * c + 1][1])};
                u32x4 o[2];
#pragma unroll
                for (int i = 0; i < 2; ++i) o[i] = *(LDSP u32x4*)(hw + (t * 16 + i * 8 + (l >> 3)) * 144 + (l & 7) * 16);
                if (t == 0) {
                    const float gv = rbf(__builtin_amdgcn_rcpf(1.f + __builtin_amdgcn_exp2f(-1.44269504f * rbf(g ? gd : a_sum[0][1]))));
                    if (cb == 0 && g < 2) st4c(__float_as_uint(gv), rws, l * 4, O_GATE + (rt * 128 + w * 32) * 4);
                }
#pragma unroll
                for (int i = 0; i < 2; ++i) st16c(o[i], rws, (l >> 3) * 1152 + (l & 7) * 16, O_H + (rt * 128 + w * 32 + t * 16 + i * 8) * 1152 + cb * 128);
                SB();
            }
            STAMP(4);
            wait_vm<0>();
            if (l == 0) st4c(tag, rws, 0, O_FA + b * 128 + w * 4);
            STAMP(5);
        }
    } else if (idx < 27) {
        const int tb = xc, c0 = (idx - 18) * 64;
        const int rg = l >> 3, c = l & 7;
        constexpr int TO = 8 * 9 * 1152;
        u32x4 sk[4];
        LDSP char* kb = lds + w * 9 * 1152 + c * 16;
        {
            const u32x4 rsk = mk_rsrc(shk, D * 8), rki = mk_rsrc(kin, S * D * 2), rvi = mk_rsrc(vin, S * D * 2);
#pragma unroll
            for (int i = 0; i < 4; ++i) ld16(sk[i], rsk, (c0 + c * 8) * 8, i * 16);
            const unsigned vo = (tb * 256 + w * 32 + (l >> 3)) * 1152 + c0 * 2 + (l & 7) * 16;
            dma16n(rki, w * 9 * 1152, vo - 9216, 0);
#pragma unroll
            for (int i = 0; i < 4; ++i) { dma16n(rki, (w * 9 + 1 + i) * 1152, vo, i * 9216); dma16n(rvi, (w * 9 + 5 + i) * 1152, vo, i * 9216); }
        }
        scalars();
        asm volatile("s_waitcnt vmcnt(9)" : "+v"(sk[0]), "+v"(sk[1]), "+v"(sk[2]), "+v"(sk[3]) :: "memory");
        f32x2 sf[4][4];
#pragma unroll
        for (int e = 0; e < 4; ++e) {
            sf[e][0] = f32x2{bflo(sk[e][0]), bflo(sk[e][2])}; sf[e][1] = f32x2{bfhi(sk[e][0]), bfhi(sk[e][2])};
            sf[e][2] = f32x2{bflo(sk[e][1]), bflo(sk[e][3])}; sf[e][3] = f32x2{bfhi(sk[e][1]), bfhi(sk[e][3])};
            asm volatile("" : "+v"(sf[e][0]), "+v"(sf[e][1]), "+v"(sf[e][2]), "+v"(sf[e][3]));
        }
        wait_vm<0>();
        STAMP(1);
        u32x4 kr[7], vr[4];
#pragma unroll
        for (int i = 0; i < 7; ++i) { const int j = rg + 1 + ((i + 1) >> 2); kr[i] = *(LDSP u32x4*)(kb + (j >> 1) * 1152 + ((j & 1) * 4 + ((i + 1) & 3)) * 128); }
#pragma unroll
        for (int i = 0; i < 4; ++i) vr[i] = *(LDSP u32x4*)(kb + (5 + (rg >> 1)) * 1152 + ((rg & 1) * 4 + i) * 128);
        LDSP char* tw = lds + TO + c * (8 * 512 + 16) + (63 - w * 8 - rg) * 8;
#pragma unroll
        for (int e = 0; e < 4; ++e) {
            f32x2 kk[7], u[4];
#pragma unroll
            for (int i = 0; i < 7; ++i) kk[i] = f32x2{bflo(kr[i][e]), bfhi(kr[i][e])};
#pragma unroll
            for (int i = 0; i < 4; ++i) u[i] = kk[i] * sf[e][3];
#pragma unroll
            for (int j = 1; j < 4; ++j)
#pragma unroll
                for (int i = 0; i < 4; ++i) u[i] = __builtin_elementwise_fma(kk[i + j], sf[e][3 - j], u[i]);
#pragma unroll
            for (int i = 0; i < 4; ++i) u[i] *= f32x2{bflo(vr[i][e]), bfhi(vr[i][e])};
#pragma unroll
            for (int z = 0; z < 2; ++z) *(LDSP u32x2*)(tw + (2 * e + z) * 512) = u32x2{pk2(u[3][z], u[2][z]), pk2(u[1][z], u[0][z])};
        }
        STAMP(2);
        lds_bar();
        STAMP(3);
        u32x4 uo[4];
#pragma unroll
        for (int i = 0; i < 4; ++i) { const int cl = i * 16 + (tid >> 5); uo[i] = *(LDSP u32x4*)(lds + TO + cl * 512 + (cl >> 3) * 16 + (tid & 31) * 16); }
#pragma unroll
        for (int i = 0; i < 4; ++i) st16c(uo[i], rws, (tid >> 5) * 4096 + (tid & 31) * 16, O_U + ((c0 + i * 16) * S + (1792 - tb * 256)) * 2);
        STAMP(4);
        if (w >= 4) { conv_k1(); wait_vm<4>(); } else wait_vm<0>();
        bar();
        if (tid < 4) st4c(tag, rws, tid * 4, O_FA + b * 128);
        STAMP(5);
    } else {
        scalars();
        if (w >= 4) conv_k1();
        else {
            __builtin_amdgcn_s_sleep(WSLEEP);
            warm(w2, D * D * 2, (idx - 27) * 256 + tid, 1280, 576 * 9, 1152, 9, 0);
            warm(w3, D * F * 2, (idx - 27) * 256 + tid, 1280, W3A * 18, 9216, 18, (xc & 3) * 2304);
        }
        STAMP(5);
    }
    if (idx < 12) {
        const int rt = 4 * xc + idx / 3, cb = idx % 3;
        const int kb0 = wrap9(cb * 3 + (idx / 3) * 2);
        if (w >= 4) {
            const int u = w - 4;
            const u32x4 rw = mk_rsrc(w2, D * D * 2), rh = mk_rsrc(ws + O_H, S * D * 2);
            const int lrow = u * 16 + (l >> 3);
            const unsigned vw0 = lrow * 1152 + cb * 384 + ((l & 7) ^ ((g & 1) << 1)) * 16, vw1 = (lrow + 8) * 1152 + cb * 384 + ((l & 7) ^ (((g & 1) | 2) << 1)) * 16;
            unsigned vh[9][2];
#pragma unroll
            for (int i = 0; i < 9; ++i)
#pragma unroll
                for (int e = 0; e < 2; ++e) {
                    const int ck = wrap9(kb0 + i) * 8 + ((l & 7) ^ (4 * e + g));
                    const int sh = ck < 18 ? -1 : ck < 36 ? 1 : ck < 54 ? -2 : 2;
                    vh[i][e] = (unsigned)((rt * 64 + lrow + 8 * e + sh) * 1152 + ck * 16);
                    asm volatile("" : "+v"(vh[i][e]));
                }
            auto issue_h = [&](int i) { dma16c(rh, HRING + (i & 3) * 8192 + u * 2048, vh[i][0], 0); dma16c(rh, HRING + (i & 3) * 8192 + u * 2048 + 1024, vh[i][1], 0); };
            auto issue_w = [&](int i) {
                const unsigned dst = (i % 5) * SL + u * 2048, so = wrap9(kb0 + i) * 73728;
#pragma unroll
                for (int c = 0; c < 3; ++c) { dma16(rw, dst + c * 8192, vw0, so + c * 128); dma16(rw, dst + 1024 + c * 8192, vw1, so + c * 128); }
            };
            bar();
            issue_h(0); issue_h(1); issue_h(2); issue_w(4);
            sfor<0, 9>([&](auto I) {
                constexpr int i = I.value;
                wait_bar<g2_after(i)>();
                if constexpr (i + 3 < 9) issue_h(i + 3);
                if constexpr (i >= 1 && i + 4 < 9) issue_w(i + 4);
            });
        } else {
            f32x4 acc[4][3];
#pragma unroll
            for (int c = 0; c < 3; ++c) {
                const int ct = w * 3 + c;
                u32x2 xv[4];
#pragma unroll
                for (int t = 0; t < 4; ++t) {
                    xv[t] = *(LDSP u32x2*)(lds + 4 * SL + (ct >> 2) * 8192 + (t * 16 + r) * 128 + ((((ct & 3) * 2 + (g >> 1)) ^ (r >> 1)) * 16) + (g & 1) * 8);
                    acc[t][c] = f32x4{bflo(xv[t][0]), bfhi(xv[t][0]), bflo(xv[t][1]), bfhi(xv[t][1])} * GS;
                }
#pragma unroll
                for (int rg = 0; rg < 2; ++rg) st16c(u32x4{xv[2 * rg][0], xv[2 * rg][1], xv[2 * rg + 1][0], xv[2 * rg + 1][1]}, rws, vl16, O_Q + ((rt * 2 + rg) * 36 + cb * 12 + ct) * 1024);
            }
            if (w == 0) {
                int t = (rt >> 1) + (l < 9 ? 0 : (rt & 1) ? 1 : -1);
                t = min(max(t, 0), 15);
                const int j = l < 9 ? l : l < 18 ? l - 9 : 0;
                await(u32x4{~tag, 0u, 0u, 0u}, tag, rws, O_FA + (((t & 1) * 9 + j) * 8 + (t >> 1)) * 128);
            }
            lds_bar();
            STAMP(6);
            LDSP char* xb[2]; LDSP char* wb[2][3];
#pragma unroll
            for (int s2 = 0; s2 < 2; ++s2) xb[s2] = opaque(lds + HRING + r * 128 + (((4 * s2 + g) ^ (r >> 1)) * 16));
#pragma unroll
            for (int hi = 0; hi < 2; ++hi)
#pragma unroll
                for (int c = 0; c < 3; ++c) { const int ct = w * 3 + c; wb[hi][c] = opaque(lds + hi * 2 * SL + (ct >> 2) * 8192 + (8 * g + q) * 128 + ((ct & 3) ^ sw) * 32 + p * 8); }
            bf16x8 xf[2][4], wf[2][3];
            sfor<0, 19>([&](auto KS) {
                constexpr int n = KS.value, i = n >> 1, s2 = n & 1, nb = n & 1, a = nb ^ 1;
                constexpr int sl = i % 5, hi = sl >= 2, off = (sl - 2 * hi) * SL;
                if constexpr (s2 == 0 && n < 18) {
                    lds_bar();
                    if constexpr (i == 0) STAMP(7);
                    if constexpr (i == 8) STAMP(8);
                }
                auto rd = [&](int e) {
                    if constexpr (n < 18) {
                        if (e == 0) wf[nb][0] = ld_tr(wb[hi][0] + off + s2 * 4096, 512);
                        else if (e < 5) xf[nb][e - 1] = *(LDSP bf16x8*)(xb[s2] + (i & 3) * 8192 + (e - 1) * 2048);
                        else wf[nb][e - 4] = ld_tr(wb[hi][e - 4] + off + s2 * 4096, 512);
                    }
                    SB();
                };
#pragma unroll
                for (int m = 0; m < 12; ++m) {
                    if constexpr (n >= 1) { acc[m & 3][m >> 2] = mfma(wf[a][m >> 2], xf[a][m & 3], acc[m & 3][m >> 2]); SB(); }
                    if (m < 7) rd(m);
                }
            });
            STAMP(9);
            const unsigned h1 = (r & 1) ? 0u : 0x3f803f80u, h2 = (r & 1) ? 0x3f803f80u : 0u;
            const bf16x8 half = __builtin_bit_cast(bf16x8, u32x4{h1, h1, h2, h2});
            f32x4 s1[2] = {}, s2[4] = {};
#pragma unroll
            for (int c = 0; c < 3; ++c)
#pragma unroll
                for (int rg = 0; rg < 2; ++rg) {
                    const f32x4 a0 = acc[2 * rg][c], a1 = acc[2 * rg + 1][c];
                    const u32x4 xo = {pk2(a0[0], a0[1]), pk2(a0[2], a0[3]), pk2(a1[0], a1[1]), pk2(a1[2], a1[3])};
                    st16c(xo, rws, vl16, O_X1 + ((rt * 2 + rg) * 36 + cb * 12 + w * 3 + c) * 1024);
                    s1[rg] = mfma(half, __builtin_bit_cast(bf16x8, xo), s1[rg]);
                    s2[2 * rg] = mfma_sq(xo[0], xo[1], s2[2 * rg]);
                    s2[2 * rg + 1] = mfma_sq(xo[2], xo[3], s2[2 * rg + 1]);
                }
#pragma unroll
            for (int rg = 0; rg < 2; ++rg) {
                const u32x4 so = {__float_as_uint(s1[rg][0]), __float_as_uint(pick4(s2[2 * rg], r & 3)), __float_as_uint(s1[rg][1]), __float_as_uint(pick4(s2[2 * rg + 1], r & 3))};
                if (g == (r >> 2)) st16c(so, rws, r * 16, O_STAT + ((rt * 2 + rg) * 12 + cb * 4 + w) * 256);
            }
            STAMP(10);
            wait_vm<0>();
            if (l == 0) st4c(tag, rws, 0, O_FB + b * 128 + w * 4);
            STAMP(11);
        }
    } else if (idx >= 14) {
        if (w >= 4) {
            wait_vm<0>();
#pragma unroll
            for (int c = 1; c < 4; ++c)
#pragma unroll
                for (int i = 0; i < 4; ++i) dma16(rk, cw * 16640 + c * 4160 + 64 + i * 1024, vl16 + 4 + 4 * c, i * 1024);
            if (l < 16) {
                const int c = l >> 2;
                u32x4 o = {0u, 0u, 0u, 0u};
                if ((l & 3) == 3) {
                    o[3] = c == 0 ? k0[0] : c == 1 ? k0[1] : c == 2 ? k0[2] : k0[3];
                    o[2] = c == 0 ? 0u : c == 1 ? k0[0] : c == 2 ? k0[1] : k0[2];
                    o[1] = c <= 1 ? 0u : c == 2 ? k0[0] : k0[1];
                    o[0] = c <= 2 ? 0u : k0[0];
                }
                *(LDSP u32x4*)(lds + cw * 16640 + c * 4160 + (l & 3) * 16) = o;
            }
            if (l < 56) *(LDSP u32x4*)(lds + CV_U + cw * 9152 + (l / 28) * 4576 + 4096 + (l % 28) * 16) = u32x4{0u, 0u, 0u, 0u};
        } else {
            await(u32x4{~tag, 0u, 0u, 0u}, tag, rws, O_FA + ((18 + (cg >> 4)) * 8 + (l & 7)) * 128);
            STAMP(6);
            u32x4 ur[4]; unsigned nx[4];
#pragma unroll
            for (int i = 0; i < 4; ++i) ld16c(ur[i], rws, vl16, O_U + ch * 4096 + i * 1024);
            const u32x4 ru = mk_rsrc(ws + O_U + ch * 4096, 4096);
#pragma unroll
            for (int i = 0; i < 4; ++i) ld4c(nx[i], ru, vl16 + 16, i * 1024);
            asm volatile("s_waitcnt vmcnt(0)" : "+v"(ur[0]), "+v"(ur[1]), "+v"(ur[2]), "+v"(ur[3]), "+v"(nx[0]), "+v"(nx[1]), "+v"(nx[2]), "+v"(nx[3]));
            LDSP char* uw = lds + CV_U + cw * 9152 + l * 16;
#pragma unroll
            for (int i = 0; i < 4; ++i) {
                *(LDSP u32x4*)(uw + 4576 + i * 1024) = ur[i];
                *(LDSP u32x4*)(uw + i * 1024) = u32x4{__builtin_amdgcn_alignbit(ur[i][1], ur[i][0], 16), __builtin_amdgcn_alignbit(ur[i][2], ur[i][1], 16),
                                                      __builtin_amdgcn_alignbit(ur[i][3], ur[i][2], 16), __builtin_amdgcn_alignbit(nx[i], ur[i][3], 16)};
            }
        }
        wait_bar_l<0>();
        STAMP(7);
        LDSP char* yi = lds + CV_Y;
        if (w < 4) {
            LDSP char* ab = lds + cw * 16640 + (r & 3) * 4160 + ((r >> 2) + g) * 16;
            LDSP char* bb = opaque(lds + CV_U + cw * 9152 + (r & 1) * 4576 + (28 - 4 * (r >> 1) + g) * 16);
            f32x4 acc[8] = {};
            bf16x8 A[2][8], B[2][8];
            auto ldf = [&](int t, int e) {
                if (e & 1) A[t & 1][e >> 1] = *(LDSP bf16x8*)(ab + (t + 8 * (e >> 1)) * 64);
                else B[t & 1][e >> 1] = *(LDSP bf16x8*)(bb + (7 - (e >> 1)) * 512 + t * 64);
            };
#pragma unroll
            for (int e = 0; e < 16; ++e) ldf(0, e);
            {
                const int j = 30 - 2 * r - 8 * g;
                u32x4 dv;
#pragma unroll
                for (int e = 0; e < 4; ++e) dv[e] = (j == 2 * e) ? dD : 0u;
                const bf16x8 da = __builtin_bit_cast(bf16x8, dv);
#pragma unroll
                for (int n = 0; n < 8; ++n) acc[n] = mfma(da, B[0][n], acc[n]);
            }
            SB();
#pragma unroll
            for (int t = 0; t < 8; ++t) {
                int cnt = 0;
#pragma unroll
                for (int m = 0; m < 8; ++m)
#pragma unroll
                    for (int n = 0; n + m < 8; ++n) {
                        acc[m + n] = mfma(A[t & 1][m], B[t & 1][n], acc[m + n]);
                        if (t < 7 && (cnt & 1) == 0 && cnt < 32) ldf(t + 1, cnt >> 1);
                        ++cnt;
                        SB();
                    }
            }
            STAMP(9);
            LDSP char* yw = opaque(yi + (r >> 1) * 272 + (8 * g + (r & 1)) * 8 + cw * 2);
#pragma unroll
            for (int c = 0; c < 8; ++c)
#pragma unroll
                for (int e = 0; e < 4; e += 2) {
                    const unsigned o = pk2(acc[c][e], acc[c][e + 1]);
                    *(LDSP u16*)(yw + c * 2176 + e * 16) = (u16)o;
                    *(LDSP u16*)(yw + c * 2176 + e * 16 + 16) = (u16)(o >> 16);
                }
        }
        else warm(w3, D * F * 2, W3A * 18 + (idx - 14) * 256 + tid - 256, 18 * 256, 576 * 18, 9216, 18, (xc & 3) * 2304);
        lds_bar();
#pragma unroll
        for (int i = 0; i < 2; ++i) {
            const int r32 = i * 32 + (tid >> 4);
            const u32x2 lo = *(LDSP u32x2*)(yi + r32 * 272 + r * 8), hi = *(LDSP u32x2*)(yi + r32 * 272 + 128 + r * 8);
            st16c(u32x4{lo[0], lo[1], hi[0], hi[1]}, rws, (tid >> 4) * 36864 + r * 16, O_Y + (i * 32 * 36 + (cg >> 2)) * 1024 + (cg & 3) * 256);
        }
        STAMP(10);
    } else {
    }
    DUMP(0, 12); }
    const int fc = 2 * (xc & 3) + (idx & 1), rt = (xc >> 2) * 16 + (idx >> 1);
    const int wi = w >> 2, wj = w & 3;
    const int rho = rt % 9;
    DECL;
    const unsigned pbase = O_X1 + ((rt * 2 + wi) * 36 + wj) * 1024;
    u32x4 sr[3], xr[9];
    const u32x4 r3 = mk_rsrc((const char*)w3 + fc * 1152 + (size_t)w * 8 * 9216, D * F * 2 - (fc * 1152 + w * 8 * 9216));
    const u32x4 r4 = mk_rsrc((const char*)w4 + (size_t)(fc * 576 + w * 8) * 1152, D * F * 2 - (fc * 576 + w * 8) * 1152);
    const unsigned vsw = ((l & 7) ^ (g << 1)) * 16;
    const unsigned vo3 = (l >> 3) * 9216 + vsw, vo4 = (l >> 3) * 1152 + vsw;
    const unsigned ldsw = w * 1024;
    auto sigma = [&](int pos) { return pos == 0 ? 8 : ((fc + pos) & 7); };
    auto issue = [&](auto N) {
        constexpr int n = N.value, st = n / 9, kk = n % 9;
        if constexpr (st < 9) dma16(r3, ldsw + (st & 1) * HALF + kk * 8192, vo3, kk * 64 * 9216 + wrap9(st + rho) * 128);
        else dma16(r4, ldsw + (st & 1) * HALF + kk * 8192, vo4, wrap9(kk + rho) * 73728 + wrap9(sigma(st - 9) + rho) * 128);
    };
    lds_bar();
    sfor<0, 9>(issue);
    if (w == 0) await(u32x4{~tag, 0u, 0u, 0u}, tag, rws, O_FB + (((rt & 3) * 3 + min(l, 2)) * 8 + (rt >> 2)) * 128);
    wait_bar<9>();
    if (idx >= 14 && tid < 4) st4c(tag, rws, tid * 4, O_FB + b * 128);
    STAMP(0);
#pragma unroll
    for (int i = 0; i < 3; ++i) ld16c(sr[i], rws, vl16, O_STAT + (rt * 2 + wi) * 3072 + i * 1024);
#pragma unroll
    for (int i = 0; i < 9; ++i) {
        if (i == 4) bar();
        ld16c(xr[i], rws, vl16, pbase + i * 4096);
    }
    LDSP char* gr = lds + wi * 4096 + l * 16;
    LDSP char* gw = lds + wi * 4096 + (wj >> 1) * 1024 + l * 16 + (wj & 1) * 8;
    float sc[2], of[2];
    asm volatile("s_waitcnt vmcnt(9)" : "+v"(sr[0]), "+v"(sr[1]), "+v"(sr[2]));
#pragma unroll
    for (int t = 0; t < 2; ++t) {
        const float s1 = sum_g(__uint_as_float(sr[0][2 * t]) + __uint_as_float(sr[1][2 * t]) + __uint_as_float(sr[2][2 * t]));
        const float s2 = sum_g(__uint_as_float(sr[0][2 * t + 1]) + __uint_as_float(sr[1][2 * t + 1]) + __uint_as_float(sr[2][2 * t + 1]));
        const float mean = s1 * (1.f / D);
        const float var = fmaxf(s2 * (1.f / D) - mean * mean, 0.f);
        sc[t] = GS * __builtin_amdgcn_rsqf(var + 1e-5f * GS * GS); of[t] = -mean * sc[t];
    }
    STAMP(1);
    LDSP char* xw = opaque(gw + HALF + 8192);
    auto putx = [&](auto I) {
        constexpr int i = I.value;
        u32x4& v = xr[i];
        asm volatile("s_waitcnt vmcnt(%1)" : "+v"(v) : "n"(8 - i));
#pragma unroll
        for (int t = 0; t < 2; ++t)
            *(LDSP u32x2*)((i == 0 ? gw + HALF : xw + (i - 1) * 8192) + t * 2048) = u32x2{norm2(v[2 * t], sc[t], of[t]), norm2(v[2 * t + 1], sc[t], of[t])};
    };
    sfor<0, 4>(putx);
    STAMP(2);
    bf16x8 X[36];
    LDSP char* tb = lds + (4 * g + q) * 128 + (wj ^ sw) * 32 + p * 8;
    LDSP char* tbs[3] = {tb, opaque(tb + 65536), opaque(tb + 131072)};
    LDSP char* grs[3] = {gr, opaque(gr + 65536), opaque(gr + 131072)};
    auto ldw = [&](auto KS) -> bf16x8 {
        constexpr int st = KS.value / 18, ks = KS.value % 18, off = (st & 1) * HALF + ks * 4096;
        return ld_tr(tbs[off >> 16] + (off & 65535), 2048);
    };
    auto ldx = [&](auto KS) {
        constexpr int gk = KS.value, ks = gk % 18;
        if constexpr (gk < 18 || (gk >= 162 && gk < 180)) {
            constexpr int off = (gk < 18 ? HALF : 0) + (ks >> 1) * 8192 + (ks & 1) * 1024;
            LDSP char* a = grs[off >> 16] + (off & 65535);
            X[2 * ks] = *(LDSP bf16x8*)a; X[2 * ks + 1] = *(LDSP bf16x8*)(a + 2048);
        }
    };
    bf16x8 Wf[3];
    unsigned Gown[9][4];
    f32x4 acc[2][2];
    GeluT T;
    auto putg = [&](int s, int t) { *(LDSP u32x2*)(gw + s * 8192 + t * 2048) = u32x2{Gown[s][2 * t], Gown[s][2 * t + 1]}; };
    const unsigned pt = O_PART + rt * 9 * 8 * 8 * 1024 + w * 1024;
    auto mine = [&](int pos) { return pt + (unsigned)((sigma(pos) * 8 + fc) * 8192); };
    auto theirs = [&](int pos) { return pt + (unsigned)((fc * 8 + ((fc - pos) & 7)) * 8192); };
    auto yflag = [&]() {
        const int c2 = l < 16 ? wrap9(fc + rho) * 16 + l : wrap9(8 + rho) * 16 + (fc & 3) * 4 + (l & 3);
        return O_FB + ((14 + c2 % 18) * 8 + c2 / 18) * 128;
    };
    auto pflag = [&]() { return O_FP + (((rt & 15) * 2 + (l & 1)) * 8 + (rt >> 4) * 4 + ((l >> 1) & 3)) * 128; };
    Side sd, se;
    u32x4 pv[8], ev[8], sel[2];
    u32x4 fl;
    unsigned gt[2];
    float base[8];

    sfor<0, 18>([&](auto ST) {
        constexpr int st = ST.value;
        sfor<0, 18>([&](auto KS) {
            constexpr int ks = KS.value, gk = st * 18 + ks, G = gk / 6, k = gk % 6;
            if constexpr (k == 0 && G <= 51) {
                static_assert(SCH.vm[G] >= 0 && SCH.vm[G] < 64, "schedule");
                if constexpr (G == G_CHK || G == G_YCHK) {
                    if (w == 0) {
                        u32x4 v = fl;
                        asm volatile("s_waitcnt vmcnt(%1)" : "+v"(v) : "n"(G == G_CHK ? SCH.pollwait : SCH.ywait));
                        await(v, tag, rws, G == G_CHK ? pflag() : yflag());
                    }
                }
                if constexpr (G <= 2 || (G >= 26 && G <= 28)) wait_bar_l<SCH.vm[G]>(); else wait_bar<SCH.vm[G]>();
                if constexpr (G == G_PUB) { if (tid < 4) st4c(tag, rws, tid * 4, O_FP + b * 128); }
                if constexpr (G == G_POLL) ld_flag(fl, w, rws, pflag());
                if constexpr (G == G_YPOLL) ld_flag(fl, w, rws, yflag());
                if constexpr (gk == 0) { ldx(IC(0)); Wf[0] = ldw(IC(0)); ldx(IC(1)); Wf[1] = ldw(IC(1)); }
            }
            if constexpr (ks == 0) STAMP(3 + st);
            SB();
            f32x4* A = acc[st & 1];
            f32x4* P = acc[(st & 1) ^ 1];
            constexpr bool late_reads = st >= 1 && G < 25;
            auto reads = [&]() { if constexpr (gk + 2 < 324) { Wf[(gk + 2) % 3] = ldw(IC(gk + 2)); ldx(IC(gk + 2)); } SB(); };
            auto dma = [&]() { if constexpr (SCH.dma[G][k] >= 0) issue(IC(SCH.dma[G][k] >= 0 ? SCH.dma[G][k] : 0)); SB(); };
            const bf16x8 Wk = Wf[gk % 3];
            mfma1<ks == 0>(A[0], Wk, X[2 * ks]);
            SB();
            if constexpr (late_reads) dma(); else reads();
            if constexpr (G == 25 || G == 26) { putg(3 * (G - 25) + k / 2, k & 1); SB(); }
            if constexpr (G == 27) { putg(6 + k / 2, k & 1); SB(); }
            mfma1<ks == 0>(A[1], Wk, X[2 * ks + 1]);
            SB();
            if constexpr (late_reads) reads(); else dma();
            if constexpr (st >= 1 && st <= 8) {
                if constexpr ((ks & 1) == 0) gelu_even<ks / 2>(T, P);
                else gelu_odd<ks / 2>(T, P, Gown[st - 1]);
            }
            if constexpr (G == 0 && (k & 1)) putx(IC(4 + k / 2));
            if constexpr (G == 1 && k < 2) putx(IC(7 + k));
            if constexpr (G == 27 && k < 4) Gown[8][k] = gelu2(P[k >> 1][(k & 1) * 2], P[k >> 1][(k & 1) * 2 + 1]);
            if constexpr (n_store(G, k)) {
                u32x4 o = {pk2(P[0][0], P[0][1]), pk2(P[0][2], P[0][3]), pk2(P[1][0], P[1][1]), pk2(P[1][2], P[1][3])};
#pragma unroll
                for (int e = 0; e < 4; ++e) o[e] = min(o[e], 0xfffffffeu);
                st16c(o, rws, vl16, mine(st - 10));
            }
            if constexpr (fill_at(G, k) >= 0) st16c(u32x4{~0u, ~0u, ~0u, ~0u}, rws, vl16, mine(fill_at(G, k)));
            if constexpr (G == 27) {
                const unsigned po = pbase + wrap9(fc + rho) * 4096;
                if constexpr (k == 0) ld16c(sd.x, rws, vl16, po);
                if constexpr (k == 1) ld16c(sd.gq, rws, vl16, po + NB);
                if constexpr (k == 2) ld16c(sd.y, rws, vl16, po + 2 * NB);
                if constexpr (k == 3) ld4c(gt[0], rws, r * 4, O_GATE + (rt * 64 + wi * 32) * 4);
                if constexpr (k == 4) ld4c(gt[1], rws, r * 4, O_GATE + (rt * 64 + wi * 32 + 16) * 4);
            }
            if constexpr ((G == 34 || G == 35) && (k == 2 || k == 5)) {
                constexpr int e = (G - 34) * 2 + (k == 5);
                mix_base(base[2 * e], base[2 * e + 1], sd.gq[e], sd.y[e], __uint_as_float(gt[e >> 1]));
            }
            if constexpr (get_at(G, k) >= 0) ld16c(pv[get_at(G, k) >= 0 ? get_at(G, k) : 0], rws, vl16, theirs(get_at(G, k)));
            if constexpr (G == 52 && k == 1) ld_shared(ev, se, w - fc, rws, vl16, pt + 64 * 8192, pbase + wrap9(8 + rho) * 4096);
            if constexpr (G == 51 && k == 2) {
                const unsigned one = (r >> 2) == g ? ((r & 1) ? 0x3f800000u : 0x3f80u) : 0u;
                sel[0] = u32x4{(r & 2) ? 0u : one, (r & 2) ? one : 0u, 0u, 0u};
                sel[1] = u32x4{0u, 0u, sel[0][0], sel[0][1]};
                u32x4* pp = pv; u32x4* ss = sel;
                asm volatile("" : "+v"(pp[1]), "+v"(pp[2]), "+v"(pp[3]), "+v"(pp[4]), "+v"(ss[0]), "+v"(ss[1]));
                const unsigned so[4] = {theirs(1), theirs(2), theirs(3), theirs(4)};
                settle<4>(pv + 1, so, rws, vl16);
            }
            if constexpr (G == 51 && k >= 2) add_piece(A[0], A[1], sel[0], sel[1], pv[k - 1]);
            if constexpr (G == 52 && k == 0) add_piece(A[0], A[1], sel[0], sel[1], sd.x);
            SB();
        });
    });
    STAMP(21);
    LDSP char* scr = lds + 2 * HALF + w * 2048;
    auto ro = __builtin_amdgcn_make_buffer_rsrc((void*)(out + (size_t)(rt * 64 + wi * 32) * D + wj * 16), 0, 32 * D * 2 - wj * 32, 0x00020000);
    const unsigned vout = (l >> 1) * (D * 2) + (l & 1) * 16;
    u32x4 ve;
    asm volatile("" : "+v"(gt[0]), "+v"(gt[1]));
    const float ngs[2] = {rbf(1.f - __uint_as_float(gt[0])) * GINV, rbf(1.f - __uint_as_float(gt[1])) * GINV};
    if (w == fc) {
        f32x4 c[2] = {};
        float be[8];
        asm volatile("s_waitcnt vmcnt(2)" : "+v"(c[0]), "+v"(c[1]), "+v"(ev[0]), "+v"(ev[1]), "+v"(ev[2]), "+v"(ev[3]), "+v"(ev[4]), "+v"(ev[5]), "+v"(ev[6]), "+v"(ev[7]));
        unsigned so[8];
#pragma unroll
        for (int i = 0; i < 8; ++i) so[i] = pt + (64 + i) * 8192;
        settle<4>(ev, so, rws, vl16); settle<4>(ev + 4, so + 4, rws, vl16);
#pragma unroll
        for (int i = 0; i < 8; ++i) add_piece(c[0], c[1], sel[0], sel[1], ev[i]);
        add_piece(c[0], c[1], sel[0], sel[1], se.x);
#pragma unroll
        for (int e = 0; e < 4; ++e) mix_base(be[2 * e], be[2 * e + 1], se.gq[e], se.y[e], __uint_as_float(gt[e >> 1]));
        asm volatile("" : "+v"(c[0]), "+v"(c[1]));
        ve = mix_out(be, ngs, c, scr, l);
    }
    {
        asm volatile("s_waitcnt vmcnt(0)" : "+v"(pv[5]), "+v"(pv[6]), "+v"(pv[7]));
        if (w == fc) __builtin_amdgcn_raw_buffer_store_b128(ve, ro, vout, wrap9(8 + rho) * 128, OAUX);
        const unsigned so[3] = {theirs(5), theirs(6), theirs(7)};
        settle<3>(pv + 5, so, rws, vl16);
        STAMP(22);
#pragma unroll
        for (int i = 5; i < 8; ++i) add_piece(acc[1][0], acc[1][1], sel[0], sel[1], pv[i]);
        asm volatile("s_nop 7\n\ts_nop 1" : "+v"(acc[1][0]), "+v"(acc[1][1]));
        __builtin_amdgcn_raw_buffer_store_b128(mix_out(base, ngs, acc[1], scr, l), ro, vout, wrap9(fc + rho) * 128, OAUX);
    }
    if (b == 0 && w < 4) {
        await(u32x4{~tag, 0u, 0u, 0u}, tag, rws, O_FP + tid * 128);
    }
    if (b == 0) {
        bar();
        if (tid == 0) st4c(tag, rws, 0, 0);
    }
    DRAIN(); STAMP(23); DUMP(16, 24);
}

extern "C" void kernel_launch(void* const* d_in, const int* in_sizes, int n_in, void* d_out, int out_size, void* d_ws, size_t ws_size, hipStream_t stream) {
    (void)hipFuncSetAttribute((const void*)k_all, hipFuncAttributeMaxDynamicSharedMemorySize, 163840);
    k_all<<<256, 512, 163840, stream>>>((const u16*)d_in[0], (const u16*)d_in[1], (const u16*)d_in[2], (const u16*)d_in[3], (const u16*)d_in[4], (const u16*)d_in[5],
                                        (const u16*)d_in[7], (const u16*)d_in[8], (const u16*)d_in[9], (const u16*)d_in[10], (const u16*)d_in[11],
                                        (char*)d_ws, (u16*)d_out);
}
```

```cpp
#include <hip/hip_runtime.h>
#include <hip/hip_bf16.h>
#include <stdint.h>
#include <type_traits>

typedef unsigned short u16;
typedef __attribute__((ext_vector_type(4))) short s16x4;
typedef __attribute__((ext_vector_type(8))) __bf16 bf16x8;
typedef __attribute__((ext_vector_type(2))) __bf16 bf16x2;
typedef __attribute__((ext_vector_type(4))) float f32x4;
typedef __attribute__((ext_vector_type(2))) float f32x2;
typedef __attribute__((ext_vector_type(4))) unsigned u32x4;
typedef __attribute__((ext_vector_type(2))) unsigned u32x2;
#define LDSP __attribute__((address_space(3)))
#define DI __device__ __forceinline__
#define SB() __builtin_amdgcn_sched_barrier(0)
#define IC(v) std::integral_constant<int, (v)>{}

constexpr int S = 2048, D = 576, F = 4608;
constexpr float GS = 0.46866869f;
constexpr float GALPHA = -4.91222954f;
constexpr float GINV = 2.13370343f;

template <int I, int N, class Fn> DI void sfor(Fn&& f) {
    if constexpr (I < N) { f(std::integral_constant<int, I>{}); sfor<I + 1, N>(f); }
}
DI float bflo(unsigned v) { return __uint_as_float(v << 16); }
DI float bfhi(unsigned v) { return __uint_as_float(v & 0xffff0000u); }
DI unsigned pk2(float a, float b) { f32x2 v = {a, b}; return __builtin_bit_cast(unsigned, __builtin_convertvector(v, bf16x2)); }
DI float rbf(float f) { return bflo(pk2(f, 0.f)); }
DI int wrap9(int v) { return v >= 9 ? v - 9 : v; }
DI u32x4 mk_rsrc(const void* p, unsigned bytes) {
    unsigned long long a = (unsigned long long)p;
    u32x4 r = {(unsigned)a, (unsigned)(a >> 32) & 0xffffu, bytes, 0x00020000u};
    return r;
}
DI void dma16(u32x4 rs, unsigned lds, unsigned voff, unsigned soff) {
    asm volatile("s_mov_b32 m0, %0\n\ts_nop 0\n\tbuffer_load_dwordx4 %1, %2, %3 offen lds" :: "s"(lds), "v"(voff), "s"(rs), "s"(soff) : "memory");
}
DI void dma16c(u32x4 rs, unsigned lds, unsigned voff, unsigned soff) {
    asm volatile("s_mov_b32 m0, %0\n\ts_nop 0\n\tbuffer_load_dwordx4 %1, %2, %3 offen sc1 lds" :: "s"(lds), "v"(voff), "s"(rs), "s"(soff) : "memory");
}
DI void dma16n(u32x4 rs, unsigned lds, unsigned voff, unsigned soff) {
    asm volatile("s_mov_b32 m0, %0\n\ts_nop 0\n\tbuffer_load_dwordx4 %1, %2, %3 offen nt lds" :: "s"(lds), "v"(voff), "s"(rs), "s"(soff) : "memory");
}
DI void ld16(u32x4& d, u32x4 rs, unsigned vo, unsigned so) { asm volatile("buffer_load_dwordx4 %0, %1, %2, %3 offen" : "=v"(d) : "v"(vo), "s"(rs), "s"(so)); }
template <int OFF> DI void ld4(unsigned& d, u32x4 rs, unsigned vo) { asm volatile("buffer_load_dword %0, %1, %2, 0 offen offset:%3" : "=v"(d) : "v"(vo), "s"(rs), "n"(OFF)); }
DI void ld16c(u32x4& d, u32x4 rs, unsigned vo, unsigned so) { asm volatile("buffer_load_dwordx4 %0, %1, %2, %3 offen sc1" : "=v"(d) : "v"(vo), "s"(rs), "s"(so)); }
DI void ld4c(unsigned& d, u32x4 rs, unsigned vo, unsigned so) { asm volatile("buffer_load_dword %0, %1, %2, %3 offen sc1" : "=v"(d) : "v"(vo), "s"(rs), "s"(so)); }
DI void st16c(u32x4 v, u32x4 rs, unsigned vo, unsigned so) { asm volatile("buffer_store_dwordx4 %0, %1, %2, %3 offen sc1\n\ts_nop 1" :: "v"(v), "v"(vo), "s"(rs), "s"(so) : "memory"); }
DI void st4c(unsigned v, u32x4 rs, unsigned vo, unsigned so) { asm volatile("buffer_store_dword %0, %1, %2, %3 offen sc1" :: "v"(v), "v"(vo), "s"(rs), "s"(so) : "memory"); }
template <int N> DI void wait_vm() { asm volatile("s_waitcnt vmcnt(%0)" :: "n"(N) : "memory"); }
DI void bar() { asm volatile("s_barrier" ::: "memory"); }
#define OAUX 0
#define FILLV 1
#define XORD 1
#define POLLV 0
#define W3W 3
#define W3SLEEP 10
#define FPACE 4
#define FSLEEP 64
#define NOW4
#define W3FULL
#define LA1 2
#define W2PACE 4
#define WSLEEP 64
#define W3A 0
#define WAUX 0
#define FAUX 16
DI void stf(void* base, unsigned bytes, unsigned vo, u32x4 v) {
    __builtin_amdgcn_raw_buffer_store_b128(v, __builtin_amdgcn_make_buffer_rsrc(base, 0, bytes, 0x00020000), vo, 0, FAUX);
}
template <int N> DI void wait_bar() { asm volatile("s_waitcnt vmcnt(%0)\n\ts_barrier" :: "n"(N) : "memory"); }
template <int N> DI void wait_bar_l() { asm volatile("s_waitcnt vmcnt(%0) lgkmcnt(0)\n\ts_barrier" :: "n"(N) : "memory"); }
DI void lds_bar() { asm volatile("s_waitcnt lgkmcnt(0)\n\ts_barrier" ::: "memory"); }
DI bf16x8 ld_tr(LDSP char* a, int second) {
    s16x4 lo = __builtin_amdgcn_ds_read_tr16_b64_v4i16((LDSP s16x4*)a);
    s16x4 hi = __builtin_amdgcn_ds_read_tr16_b64_v4i16((LDSP s16x4*)(a + second));
    return __builtin_bit_cast(bf16x8, __builtin_shufflevector(lo, hi, 0, 1, 2, 3, 4, 5, 6, 7));
}
DI void pin(unsigned& v) { asm volatile("" : "+v"(v)); }
DI LDSP char* opaque(LDSP char* p) { asm volatile("" : "+v"(p)); return p; }
DI f32x4 mfma(bf16x8 a, bf16x8 b, f32x4 c) { return __builtin_amdgcn_mfma_f32_16x16x32_bf16(a, b, c, 0, 0, 0); }
DI float sum_g(float v) {
    v += __shfl_xor(v, 16); v += __shfl_xor(v, 32); return v;
}
DI float gelu_f(float x) {
    const float u = x * GS;
    const float e = __builtin_amdgcn_exp2f(u * __builtin_fmaf(-u, u, GALPHA));
    return x * __builtin_amdgcn_rcpf(1.f + e);
}
template <bool FIRST> DI void mfma2(f32x4& c0, f32x4& c1, const bf16x8& a, const bf16x8& b0, const bf16x8& b1) {
    if constexpr (FIRST) asm volatile("v_mfma_f32_16x16x32_bf16 %0, %2, %3, 0\n\tv_mfma_f32_16x16x32_bf16 %1, %2, %4, 0" : "=&v"(c0), "=&v"(c1) : "v"(a), "v"(b0), "v"(b1));
    else asm volatile("v_mfma_f32_16x16x32_bf16 %0, %2, %3, %0\n\tv_mfma_f32_16x16x32_bf16 %1, %2, %4, %1" : "+v"(c0), "+v"(c1) : "v"(a), "v"(b0), "v"(b1));
}
template <bool FIRST> DI void mfma1(f32x4& c0, const bf16x8& a, const bf16x8& b0) {
    if constexpr (FIRST) asm volatile("v_mfma_f32_16x16x32_bf16 %0, %1, %2, 0" : "=&v"(c0) : "v"(a), "v"(b0));
    else asm volatile("v_mfma_f32_16x16x32_bf16 %0, %1, %2, %0" : "+v"(c0) : "v"(a), "v"(b0));
}
#define STAMP(i) do {} while (0)
#define DUMP(kid, n) do {} while (0)
#define DECL do {} while (0)
#define DRAIN() do {} while (0)
struct GeluT { float t0, t1, t2; };
template <int e> DI void gelu_even(GeluT& T, const f32x4* P) {
    if constexpr (e == 0) asm volatile("v_fma_f32 %0, -%1, %1, %2" : "=v"(T.t0) : "v"(P[0][0]), "s"(GALPHA));
    else if constexpr (e < 8) asm volatile("v_add_f32 %1, 1.0, %0\n\tv_fma_f32 %0, -%2, %2, %3\n\tv_rcp_f32 %1, %1" : "+v"(T.t0), "=&v"(T.t1) : "v"(P[e >> 2][e & 3]), "s"(GALPHA));
    else asm volatile("v_add_f32 %0, 1.0, %1\n\ts_nop 0\n\tv_rcp_f32 %0, %0" : "=&v"(T.t1) : "v"(T.t0));
}
template <int e> DI void gelu_odd(GeluT& T, const f32x4* P, unsigned* Go) {
    if constexpr (e == 0) asm volatile("v_mul_f32 %0, %0, %1\n\ts_nop 0\n\tv_exp_f32 %0, %0" : "+v"(T.t0) : "v"(P[0][0]));
    else if constexpr (e < 8 && (e & 1)) asm volatile("v_mul_f32 %0, %0, %3\n\tv_mul_f32 %1, %2, %4\n\tv_exp_f32 %0, %0" : "+v"(T.t0), "=&v"(T.t2) : "v"(T.t1), "v"(P[e >> 2][e & 3]), "v"(P[(e - 1) >> 2][(e - 1) & 3]));
    else if constexpr (e < 8) asm volatile("v_mul_f32 %0, %0, %4\n\tv_mul_f32 %1, %1, %5\n\tv_exp_f32 %0, %0\n\tv_cvt_pk_bf16_f32 %2, %3, %1" : "+v"(T.t0), "+v"(T.t1), "=&v"(Go[(e - 1) >> 1]) : "v"(T.t2), "v"(P[e >> 2][e & 3]), "v"(P[(e - 1) >> 2][(e - 1) & 3]));
    else asm volatile("v_mul_f32 %0, %0, %3\n\ts_nop 0\n\tv_cvt_pk_bf16_f32 %1, %2, %0" : "+v"(T.t1), "=&v"(Go[3]) : "v"(T.t2), "v"(P[1][3]));
}

constexpr int HALF = 73728;
#define MAXPG1 6
#define MAXPG2 6
#define PRE 4
#define ARR1 2
#define ARR2 0
struct Sched { int dma[54][6]; int vm[54]; int ywait, pollwait, endwait; };
constexpr int avail(int n) { return n < 9 ? 0 : n < 18 ? 1 + (n - 9) / 3 : (n >= 90 && n < 99) ? 28 + (n - 90) / 3 : (n - 18) / 3 + 1; }
#define GPO 25
#define FILLG 7
constexpr int G_PUB = 42, G_POLL = 45, G_CHK = 48, G_YPOLL = GPO, G_YCHK = 27;
constexpr int fill_at(int G, int k) {
    return FILLG == 25 ? (((G == 25 || G == 26) && (k == 1 || k == 4)) ? 4 + (G - 25) * 2 + (k == 4) : -1)
                       : ((k == 1 && G >= 7 && G <= 19 && (G - 7) % 4 == 0) ? 4 + (G - 7) / 4 : -1);
}
constexpr int n_store(int G, int k) { return (G >= 30 && G % 3 == 0 && k == 1) ? 1 : 0; }
constexpr int get_at(int G, int k) {
    return (G == 53 && k == 3) ? 7 : (G == 51 && k == 5) ? 6 : (G == 48 && (k == 3 || k == 5)) ? 1 + (k == 5) : (G == 49 && (k & 1)) ? 3 + k / 2 : -1;
}
constexpr int n_other(int G, int k) { return (fill_at(G, k) >= 0) + (get_at(G, k) >= 0) + ((G == 27 && k < 5) ? 1 : 0); }
constexpr Sched make_sched() {
    Sched s{};
    int next = 9;
    for (int G = 0; G < 54; ++G) {
        int cnt = 0; const int mx = G < 27 ? MAXPG1 : MAXPG2;
        while (next + cnt < 162 && avail(next + cnt) <= G && cnt < mx) ++cnt;
        for (int k = 0; k < 6; ++k) s.dma[G][k] = -1;
        for (int c = 0; c < cnt; ++c) s.dma[G][G == 1 ? 2 + c : cnt <= 3 ? 2 * c : c] = next + c;
        next += cnt;
    }
    int pos_dma[162] = {}, before[55] = {}, pos_st3 = 0, pos_get3 = 0;
    int p = 0;
    for (int n = 0; n < 9; ++n) pos_dma[n] = p++;
    p += 12;
    for (int G = 0; G < 54; ++G) {
        before[G] = p;
        for (int k = 0; k < 6; ++k) {
            if (s.dma[G][k] >= 0) pos_dma[s.dma[G][k]] = p++;
            if (G == 39 && k == 1) pos_st3 = p;
            if (get_at(G, k) == 3) pos_get3 = p;
            p += n_store(G, k) + n_other(G, k);
        }
    }
    for (int G = 0; G < 54; ++G) {
        int need = 3 * G + 3; if (need > 161 || G == 51) need = 161;
        int last = pos_dma[need];
        if (G == G_PUB && pos_st3 > last) last = pos_st3;
        s.vm[G] = last < before[G] ? before[G] - 1 - last : -1;
    }
    s.ywait = before[G_YCHK] - before[G_YPOLL];
    s.pollwait = before[G_CHK] - before[G_POLL];
    s.endwait = before[53] - 1 - pos_get3;
    return s;
}
constexpr Sched SCH = make_sched();

DI unsigned norm2(unsigned v, float sc, float of) {
    float a, b; unsigned o;
    asm volatile("v_lshlrev_b32 %0, 16, %3\n\tv_and_b32 %1, 0xffff0000, %3\n\tv_fma_f32 %0, %0, %4, %5\n\tv_fma_f32 %1, %1, %4, %5\n\ts_nop 0\n\tv_cvt_pk_bf16_f32 %2, %0, %1"
                 : "=&v"(a), "=&v"(b), "=v"(o) : "v"(v), "v"(sc), "v"(of));
    return o;
}
DI unsigned gelu2(float u0, float u1) {
    float a, b; unsigned o;
    asm volatile("v_fma_f32 %0, -%3, %3, %5\n\tv_fma_f32 %1, -%4, %4, %5\n\tv_mul_f32 %0, %0, %3\n\tv_mul_f32 %1, %1, %4\n\tv_exp_f32 %0, %0\n\tv_exp_f32 %1, %1\n\t"
                 "v_add_f32 %0, 1.0, %0\n\tv_add_f32 %1, 1.0, %1\n\tv_rcp_f32 %0, %0\n\tv_rcp_f32 %1, %1\n\tv_mul_f32 %0, %0, %3\n\tv_mul_f32 %1, %1, %4\n\ts_nop 0\n\t"
                 "v_cvt_pk_bf16_f32 %2, %0, %1" : "=&v"(a), "=&v"(b), "=v"(o) : "v"(u0), "v"(u1), "s"(GALPHA));
    return o;
}
DI void touch(int skip, u32x4 rs, unsigned vo, unsigned& sink) {
    asm volatile("s_cmp_lg_u32 %1, 0\n\ts_cbranch_scc1 1f\n\tbuffer_load_dword %0, %3, %2, 0 offen\n\t1:" : "+v"(sink) : "s"(skip), "s"(rs), "v"(vo) : "scc");
}
struct Side { u32x4 x, gq, y; };
DI void mix_base(float& b0, float& b1, unsigned qq, unsigned y, float gv) {
    float t0, t1;
    asm volatile("v_lshlrev_b32 %0, 16, %4\n\tv_and_b32 %1, 0xffff0000, %4\n\tv_lshlrev_b32 %2, 16, %5\n\tv_and_b32 %3, 0xffff0000, %5\n\t"
                 "v_mul_f32 %0, %0, %2\n\tv_mul_f32 %1, %1, %3\n\tv_mul_f32 %0, %0, %6\n\tv_mul_f32 %1, %1, %6"
                 : "=&v"(b0), "=&v"(b1), "=&v"(t0), "=&v"(t1) : "v"(qq), "v"(y), "v"(gv));
}
DI u32x4 mix_out(const float* base, const float* ngs, const f32x4* sum, LDSP char* scr, int l) {
#pragma unroll
    for (int t = 0; t < 2; ++t)
        *(LDSP u32x2*)(scr + (t * 16 + (l & 15)) * 48 + (l >> 4) * 8) =
            u32x2{pk2(base[4 * t] + ngs[t] * sum[t][0], base[4 * t + 1] + ngs[t] * sum[t][1]), pk2(base[4 * t + 2] + ngs[t] * sum[t][2], base[4 * t + 3] + ngs[t] * sum[t][3])};
    asm volatile("s_waitcnt lgkmcnt(0)" ::: "memory");
    return *(LDSP u32x4*)(scr + (l >> 1) * 48 + (l & 1) * 16);
}
DI void add_piece(f32x4& c0, f32x4& c1, const u32x4& s0, const u32x4& s1, const u32x4& v) {
    asm volatile("v_mfma_f32_16x16x32_bf16 %0, %2, %4, %0\n\tv_mfma_f32_16x16x32_bf16 %1, %3, %4, %1" : "+v"(c0), "+v"(c1) : "v"(s0), "v"(s1), "v"(v));
}
DI unsigned umax4(const u32x4& v) { return max(max(v[0], v[1]), max(v[2], v[3])); }
template <int N> DI void settle(u32x4* v, const unsigned* so, u32x4 rp, unsigned vo) {
#pragma unroll 1
    for (int it = 0; it < (1 << 20); ++it) {
        unsigned m = 0;
#pragma unroll
        for (int i = 0; i < N; ++i) m = max(m, umax4(v[i]));
        if (__builtin_amdgcn_ballot_w64(m == 0xffffffffu) == 0) break;
        if constexpr (N == 3)
            asm volatile("buffer_load_dwordx4 %0, %3, %4, %5 offen sc1\n\tbuffer_load_dwordx4 %1, %3, %4, %6 offen sc1\n\t"
                         "buffer_load_dwordx4 %2, %3, %4, %7 offen sc1\n\ts_waitcnt vmcnt(0)"
                         : "=&v"(v[0]), "=&v"(v[1]), "=&v"(v[2]) : "v"(vo), "s"(rp), "s"(so[0]), "s"(so[1]), "s"(so[2]) : "memory");
        else
            asm volatile("buffer_load_dwordx4 %0, %4, %5, %6 offen sc1\n\tbuffer_load_dwordx4 %1, %4, %5, %7 offen sc1\n\t"
                         "buffer_load_dwordx4 %2, %4, %5, %8 offen sc1\n\tbuffer_load_dwordx4 %3, %4, %5, %9 offen sc1\n\ts_waitcnt vmcnt(0)"
                         : "=&v"(v[0]), "=&v"(v[1]), "=&v"(v[2]), "=&v"(v[3]) : "v"(vo), "s"(rp), "s"(so[0]), "s"(so[1]), "s"(so[2]), "s"(so[3]) : "memory");
    }
    asm volatile("s_nop 1");
}
DI void ld_shared(u32x4* ev, Side& se, int skip, u32x4 rs, unsigned vo, unsigned so, unsigned po) {
    unsigned t;
    asm volatile("s_cmp_lg_u32 %12, 0\n\ts_cbranch_scc1 1f\n\t"
                 "buffer_load_dwordx4 %0, %14, %13, %15 offen sc1\n\ts_add_u32 %11, %15, 0x2000\n\t"
                 "buffer_load_dwordx4 %1, %14, %13, %11 offen sc1\n\ts_add_u32 %11, %15, 0x4000\n\t"
                 "buffer_load_dwordx4 %2, %14, %13, %11 offen sc1\n\ts_add_u32 %11, %15, 0x6000\n\t"
                 "buffer_load_dwordx4 %3, %14, %13, %11 offen sc1\n\ts_add_u32 %11, %15, 0x8000\n\t"
                 "buffer_load_dwordx4 %4, %14, %13, %11 offen sc1\n\ts_add_u32 %11, %15, 0xa000\n\t"
                 "buffer_load_dwordx4 %5, %14, %13, %11 offen sc1\n\ts_add_u32 %11, %15, 0xc000\n\t"
                 "buffer_load_dwordx4 %6, %14, %13, %11 offen sc1\n\ts_add_u32 %11, %15, 0xe000\n\t"
                 "buffer_load_dwordx4 %7, %14, %13, %11 offen sc1\n\t"
                 "buffer_load_dwordx4 %8, %14, %13, %16 offen sc1\n\ts_add_u32 %11, %16, %17\n\t"
                 "buffer_load_dwordx4 %9, %14, %13, %11 offen sc1\n\ts_add_u32 %11, %11, %17\n\t"
                 "buffer_load_dwordx4 %10, %14, %13, %11 offen sc1\n\t"
                 "1:"
                 : "=&v"(ev[0]), "=&v"(ev[1]), "=&v"(ev[2]), "=&v"(ev[3]), "=&v"(ev[4]), "=&v"(ev[5]), "=&v"(ev[6]), "=&v"(ev[7]),
                   "=&v"(se.x), "=&v"(se.gq), "=&v"(se.y), "=&s"(t)
                 : "s"(skip), "s"(rs), "v"(vo), "s"(so), "s"(po), "n"(S * D * 2) : "scc");
}
DI void ld_flag(u32x4& v, int skip, u32x4 rs, unsigned vf) {
    asm volatile("s_cmp_lg_u32 %1, 0\n\ts_cbranch_scc1 1f\n\tbuffer_load_dwordx4 %0, %3, %2, 0 offen sc1\n\t1:" : "=&v"(v) : "s"(skip), "s"(rs), "v"(vf) : "scc");
}
DI bool all_set(const u32x4& v, unsigned tag) { return __builtin_amdgcn_ballot_w64(((v[0] ^ tag) | (v[1] ^ tag) | (v[2] ^ tag) | (v[3] ^ tag)) != 0u) == 0; }
DI void await(u32x4 v, unsigned tag, u32x4 rs, unsigned vf) {
#pragma unroll 1
    for (int it = 0; it < (1 << 17); ++it) {
        if (all_set(v, tag)) break;
        if (it) __builtin_amdgcn_s_sleep(1);
        asm volatile("buffer_load_dwordx4 %0, %1, %2, 0 offen sc1\n\ts_waitcnt vmcnt(0)" : "=v"(v) : "v"(vf), "s"(rs) : "memory");
    }
}

DI void sld3(unsigned& a, u32x4& b4, unsigned& c, const void* pa, const void* pb, const void* pc) {
    asm volatile("s_load_dword %0, %3, 0x0\n\ts_load_dwordx4 %1, %4, 0x0\n\ts_load_dword %2, %5, 0x0\n\ts_waitcnt lgkmcnt(0)"
                 : "=&s"(a), "=&s"(b4), "=&s"(c) : "s"(pa), "s"(pb), "s"(pc) : "memory");
}

constexpr unsigned NB = S * D * 2;
constexpr unsigned O_FA = 4096, O_FB = O_FA + 32768, O_FP = O_FB + 32768, O_GATE = O_FP + 32768, O_STAT = O_GATE + 8192;
constexpr unsigned O_X1 = 524288, O_Q = O_X1 + NB, O_Y = O_Q + NB, O_H = O_Y + NB, O_U = O_H + NB, O_PART = O_U + NB, WS_END = O_PART + 8 * NB;
constexpr int SL = 24576, HRING = 5 * SL, AUXO = HRING + 32768;
constexpr int CV_U = 4 * 16640, CV_Y = CV_U + 4 * 9152;
constexpr int g2_after(int i) {
    int pos_h[9] = {}, pos_w[9] = {}, p = 0, before = 0;
    for (int j = 0; j < 4; ++j) pos_w[j] = -1;
    for (int j = 0; j < 4; ++j) { p += 2; pos_h[j] = p - 1; }
    for (int j = 0; j <= i; ++j) {
        before = p;
        if (j >= 2 && j + 2 < 9) { p += 2; pos_h[j + 2] = p - 1; }
        if (j == 0) { p += 6; pos_w[4] = p - 1; }
        if (j >= 2 && j + 3 < 9) { p += 6; pos_w[j + 3] = p - 1; }
    }
    const int last = pos_h[i] > pos_w[i] ? pos_h[i] : pos_w[i];
    return before - 1 - last;
}
typedef __attribute__((ext_vector_type(4))) __bf16 bf16x4;
DI f32x4 mfma_sq(unsigned a, unsigned b, f32x4 c) {
    const s16x4 v = __builtin_bit_cast(s16x4, u32x2{a, b});
    return __builtin_amdgcn_mfma_f32_16x16x16bf16_1k(v, v, c, 0, 0, 0);
}
template <int V> DI void mfma_if(f32x4& c, const bf16x8& a, const u32x4& b4, int sel) {
    asm volatile("s_cmp_lg_u32 %3, %4\n\ts_cbranch_scc1 1f\n\tv_mfma_f32_16x16x32_bf16 %0, %1, %2, %0\n\t1:" : "+v"(c) : "v"(a), "v"(b4), "s"(sel), "n"(V) : "scc");
}
DI float pick4(const f32x4& v, int i) { return i == 0 ? v[0] : i == 1 ? v[1] : i == 2 ? v[2] : v[3]; }
DI void warm(const void* base, unsigned bytes, int first, int step, int end, int pitch, int per, int col) {
    auto rs = __builtin_amdgcn_make_buffer_rsrc((void*)base, 0, bytes, 0x00020000);
    unsigned a = 0;
#pragma unroll 1
    for (int ln = first; ln < end; ln += step) a |= __builtin_amdgcn_raw_buffer_load_b32(rs, (ln / per) * pitch + col + (ln % per) * 128, 0, 0);
    asm volatile("" :: "v"(a));
}

__global__ __launch_bounds__(512, 2) void k_all(const u16* __restrict__ x, const u16* __restrict__ kin, const u16* __restrict__ vin, const u16* __restrict__ shk,
                                                const u16* __restrict__ ssk, const u16* __restrict__ ssD, const u16* __restrict__ w1, const u16* __restrict__ w2,
                                                const u16* __restrict__ w3, const u16* __restrict__ w4, const u16* __restrict__ gwt,
                                                char* __restrict__ ws, u16* __restrict__ out) {
    extern __shared__ __attribute__((aligned(16))) char smem[];
    LDSP char* lds = (LDSP char*)smem;
    const int tid = threadIdx.x, l = tid & 63;
    const int w = __builtin_amdgcn_readfirstlane(tid >> 6);
    const int b = blockIdx.x, xc = b & 7, idx = b >> 3;
    const int g = l >> 4, q = (l >> 2) & 3, p = l & 3, r = l & 15;
    const unsigned vl16 = l * 16;
    unsigned sink;
    {
        unsigned long long pc; unsigned len;
        asm volatile("s_getpc_b64 %0\nk_all_code_lo:\n\ts_mov_b32 %1, k_all_code_hi-k_all_code_lo" : "=s"(pc), "=s"(len));
        const u32x4 rc = {(unsigned)pc, (unsigned)(pc >> 32) & 0xffffu, len, 0x00020000u};
        asm volatile("s_cmp_lg_u32 %1, 3\n\ts_cbranch_scc1 1f\n\tbuffer_load_dword %0, %3, %2, 0 offen\n\t1:" : "=&v"(sink) : "s"(w), "s"(rc), "v"((l * 32 + idx) * 128) : "scc");
        SB();
    }
    asm volatile("" : "+s"(x), "+s"(kin), "+s"(vin), "+s"(shk), "+s"(ssk), "+s"(ssD), "+s"(w1), "+s"(w2), "+s"(w3), "+s"(w4), "+s"(gwt), "+s"(ws), "+s"(out));
    const u32x4 rws = mk_rsrc(ws, WS_END);
    unsigned tag;
    const int sw = ((g & 1) << 1) | (q >> 1);
    const int u = w & 3, lrow = u * 16 + (l >> 3);
    const unsigned vx0 = lrow * 1152 + (((l & 7) ^ g) * 16), vx1 = (lrow + 8) * 1152 + (((l & 7) ^ (4 + g)) * 16);
    const unsigned vw0 = lrow * 1152 + ((l & 7) ^ ((g & 1) << 1)) * 16, vw1 = (lrow + 8) * 1152 + ((l & 7) ^ (((g & 1) | 2) << 1)) * 16;
    const u32x4 rw2 = mk_rsrc(w2, D * D * 2);
    const int kb2 = wrap9((idx % 3) * 3 + ((idx / 3) & 3) * 2);
    auto issue_w2 = [&](int i) {
        const unsigned dst = (i % 5) * SL + u * 2048, so = wrap9(kb2 + i) * 73728 + (idx % 3) * 384;
#pragma unroll
        for (int c = 0; c < 3; ++c) { dma16(rw2, dst + c * 8192, vw0, so + c * 128); dma16(rw2, dst + 1024 + c * 8192, vw1, so + c * 128); }
    };
    const int cg = xc * 18 + idx - 14, cw = w & 3, ch = cg * 4 + cw;
    const u32x4 rk = mk_rsrc(ssk + (size_t)(idx >= 14 ? ch : 0) * S, S * 2);
    u32x4 k0; unsigned dD;
    auto scalars = [&]() {
        const int c2 = idx >= 14 ? ch : 0;
        sld3(tag, k0, dD, ws, ssk + (size_t)c2 * S, ssD + (c2 & ~1));
        tag = tag * 1664525u + 1013904223u;
        dD = (c2 & 1) ? dD >> 16 : dD & 0xffffu;
    };
    auto conv_k1 = [&]() {
#pragma unroll
        for (int i = 0; i < 4; ++i) dma16n(rk, cw * 16640 + 64 + i * 1024, vl16 + 4, i * 1024);
    };
    const int fc = 2 * (xc & 3) + (idx & 1), rt3 = (xc >> 2) * 16 + (idx >> 1);
    unsigned vh[9][2], vfh;
    auto h_addr = [&]() {
        const int rt = 4 * xc + idx / 3;
#pragma unroll
        for (int i = 0; i < 9; ++i)
#pragma unroll
            for (int e = 0; e < 2; ++e) {
                const int ck = wrap9(kb2 + i) * 8 + ((l & 7) ^ (4 * e + g));
                const int sh = ck < 18 ? -1 : ck < 36 ? 1 : ck < 54 ? -2 : 2;
                vh[i][e] = (unsigned)((rt * 64 + lrow + 8 * e + sh) * 1152 + ck * 16);
                pin(vh[i][e]);
            }
        const int t = min(max((rt >> 1) + (l < 9 ? 0 : (rt & 1) ? 1 : -1), 0), 15), j = l < 9 ? l : l < 18 ? l - 9 : 0;
        vfh = O_FA + (((t & 1) * 9 + j) * 8 + (t >> 1)) * 128;
        pin(vfh);
    };
    { DECL; STAMP(0);
    if (idx < 18) {
        const int rt = 2 * xc + idx / 9, cb = idx % 9;
        const int kb0 = wrap9(cb + (idx / 9) * 4);
        if (w >= 4) {
            const u32x4 rx = mk_rsrc(x + (size_t)rt * 128 * D, 128 * D * 2), rw = mk_rsrc(w1, D * D * 2);
            auto issue = [&](int i) {
                const int kb = wrap9(kb0 + i);
                const unsigned dst = (i % 5) * SL + u * 2048;
                dma16(rx, dst, vx0, kb * 128); dma16(rx, dst + 1024, vx1, kb * 128);
                dma16(rw, dst + 16384, vw0, kb * 73728 + cb * 128); dma16(rw, dst + 16384 + 1024, vw1, kb * 73728 + cb * 128);
                dma16(rx, dst + 8192, vx0, kb * 128 + 64 * 1152); dma16(rx, dst + 8192 + 1024, vx1, kb * 128 + 64 * 1152);
            };
            issue(0); bar(); issue(1);
            h_addr();
            scalars();
            sfor<0, 7>([&](auto I) { wait_bar<6>(); issue(I.value + 2); });
            if (idx < 12) {
                const u32x4 rr = mk_rsrc(x + (size_t)(4 * xc + idx / 3) * 64 * D, 64 * D * 2);
                wait_bar<6>();
#pragma unroll
                for (int c = 0; c < 3; ++c) { dma16(rr, 4 * SL + u * 2048 + c * 8192, vx0, (idx % 3) * 384 + c * 128); dma16(rr, 4 * SL + u * 2048 + 1024 + c * 8192, vx1, (idx % 3) * 384 + c * 128); }
                wait_bar<6>();
                issue_w2(0);
                wait_bar<6>();
                issue_w2(1);
                bar();
                issue_w2(2); issue_w2(3);
                bar();
            } else {
                wait_bar<6>();
                wait_bar<0>();
                bar();
                if (idx >= 14) conv_k1();
            }
        } else {
            if (w < 2) dma16(mk_rsrc(gwt, D * 2), AUXO + w * 1024, vl16, w * 1024);
            bar();
            scalars();
            if (tid < 4) *(LDSP unsigned*)(lds + AUXO + 2048 + tid * 4) = 0x3f803f80u;
            LDSP char* xb[2][2]; LDSP char* wb[2][4];
#pragma unroll
            for (int hi = 0; hi < 2; ++hi) {
#pragma unroll
                for (int s2 = 0; s2 < 2; ++s2) xb[hi][s2] = opaque(lds + hi * 2 * SL + (w * 32 + r) * 128 + (((4 * s2 + g) ^ (r >> 1)) * 16));
#pragma unroll
                for (int c = 0; c < 4; ++c) wb[hi][c] = opaque(lds + hi * 2 * SL + 16384 + (8 * g + q) * 128 + (((c + w) & 3) ^ sw) * 32 + p * 8);
            }
            LDSP char* ob[9];
#pragma unroll
            for (int i = 0; i < 9; ++i) ob[i] = opaque(lds + AUXO + (r == 1 ? g * 16 + wrap9(kb0 + i) * 128 : 2048));
            u32x4 on = {0x3f803f80u, 0x3f803f80u, 0x3f803f80u, 0x3f803f80u};
            if (tid >= 16 && tid < 20) *(LDSP unsigned*)(lds + AUXO + 2048 + tid * 4) = 0x3f803f80u;
            if (tid == 8) *(LDSP unsigned*)(lds + AUXO + 2048 + 32) = 0u;
            LDSP float* st = (LDSP float*)(lds + AUXO + 4096);
            LDSP char* str_[4]; LDSP char* hwp[4];
            LDSP char* hw = lds + HRING + w * 32 * 144;
#pragma unroll
            for (int c = 0; c < 4; ++c) { str_[c] = opaque((LDSP char*)(st + ((c + w) & 3) * 16 + g * 4)); hwp[c] = opaque(hw + r * 144 + ((c + w) & 3) * 32 + g * 8); }
            LDSP char* hrp = opaque(hw + (l >> 3) * 144 + (l & 7) * 16);
            unsigned vho = (l >> 3) * 1152 + (l & 7) * 16, vdg = ((r >> 2) * 16 + r) * 4, sho = O_H + (rt * 128 + w * 32) * 1152 + cb * 128;
            asm volatile("" : "+v"(vho), "+v"(vdg), "+v"(on));
            asm volatile("" : "+s"(sho));
            f32x4 acc[2][4] = {}, a_sum[2] = {}, a_sq[2] = {}, a_cs = {};
            asm volatile("s_waitcnt vmcnt(0)" : "+v"(acc[0][0]), "+v"(acc[0][1]), "+v"(acc[0][2]), "+v"(acc[0][3]), "+v"(acc[1][0]), "+v"(acc[1][1]), "+v"(acc[1][2]), "+v"(acc[1][3]),
                         "+v"(a_sum[0]), "+v"(a_sum[1]), "+v"(a_sq[0]), "+v"(a_sq[1]), "+v"(a_cs));
            bf16x8 xf[2][2], of[2];
            s16x4 wh[2][4][2];
            float sq[2], gd;
            sfor<0, 19>([&](auto KS) {
                constexpr int n = KS.value, i = n >> 1, s2 = n & 1, nb = n & 1, a = nb ^ 1;
                constexpr int sl = i % 5, hi = sl >= 2, off = (sl - 2 * hi) * SL;
                if constexpr (s2 == 0 && n < 18) {
                    if constexpr (i == 0) lds_bar(); else bar();
                    if constexpr (i == 0) STAMP(1);
                    if constexpr (i == 8) STAMP(2);
                }
                auto rd = [&](int e) {
                    if constexpr (n < 18) {
                        if (e < 2) xf[nb][e] = *(LDSP bf16x8*)(xb[hi][s2] + off + e * 2048);
                        else if (e == 2) of[nb] = *(LDSP bf16x8*)(ob[i] + s2 * 64);
                        else if (e < 11) wh[nb][(e - 3) >> 1][(e - 3) & 1] = __builtin_amdgcn_ds_read_tr16_b64_v4i16((LDSP s16x4*)(wb[hi][(e - 3) >> 1] + off + s2 * 4096 + ((e - 3) & 1) * 512));
                    } else {
                        if (e == 8) {
                            asm volatile("" : "+v"(a_cs), "+v"(a_sq[0]), "+v"(a_sq[1]), "+v"(a_sum[1]));
                            if (r == 0) *(LDSP f32x4*)str_[0] = a_cs;
                        }
                        if (e == 9) sq[0] = __int_as_float(__builtin_amdgcn_ds_bpermute(vdg, __float_as_int(pick4(a_sq[0], r & 3))));
                        if (e == 10) sq[1] = __int_as_float(__builtin_amdgcn_ds_bpermute(vdg, __float_as_int(pick4(a_sq[1], r & 3))));
                        if (e == 11) gd = __int_as_float(__builtin_amdgcn_ds_bpermute(r * 4, __float_as_int(a_sum[1][1])));
                    }
                    SB();
                };
                auto wfr = [&](int c) { return __builtin_bit_cast(bf16x8, __builtin_shufflevector(wh[a][c][0], wh[a][c][1], 0, 1, 2, 3, 4, 5, 6, 7)); };
#pragma unroll
                for (int m = 0; m < 13; ++m) {
                    if constexpr (n >= 1) {
                        if (m < 2) mfma1<false>(a_sq[m], xf[a][m], xf[a][m]);
                        else if (m < 4) mfma1<false>(a_sum[m - 2], of[a], xf[a][m - 2]);
                        else if (m == 4) mfma1<false>(a_cs, wfr(0), __builtin_bit_cast(bf16x8, on));
                        else mfma1<false>(acc[(m - 5) & 1][(m - 5) >> 1], wfr((m - 5) >> 1), xf[a][(m - 5) & 1]);
                        if (n == 18 && m == 8) asm volatile("s_nop 4");
                        SB();
                    }
                    rd(m);
                }
            });
            lds_bar();
            STAMP(3);
            f32x4 cs[4];
#pragma unroll
            for (int c = 0; c < 4; ++c) cs[c] = *(LDSP f32x4*)str_[c];
            asm volatile("s_nop 4" : "+v"(acc[0][0]), "+v"(acc[0][1]), "+v"(acc[0][2]), "+v"(acc[0][3]), "+v"(acc[1][0]), "+v"(acc[1][1]), "+v"(acc[1][2]), "+v"(acc[1][3]),
                         "+v"(a_sum[0]), "+v"(gd));
            float mr[2], rs[2];
#pragma unroll
            for (int t = 0; t < 2; ++t) {
                const float mean = a_sum[t][0] * (1.f / D);
                rs[t] = GS * __builtin_amdgcn_rsqf(fmaxf(sq[t] * (1.f / D) - mean * mean, 0.f) + 1e-5f);
                mr[t] = -mean * rs[t];
            }
            const f32x2 al = {GALPHA, GALPHA};
            u32x4 o[4];
            SB();
#pragma unroll
            for (int t = 0; t < 2; ++t) {
                f32x2 uu[8], z[8];
#pragma unroll
                for (int e = 0; e < 8; ++e) uu[e] = f32x2{cs[e >> 1][(e & 1) * 2], cs[e >> 1][(e & 1) * 2 + 1]} * mr[t];
                SB();
#pragma unroll
                for (int e = 0; e < 8; ++e) uu[e] = __builtin_elementwise_fma(f32x2{acc[t][e >> 1][(e & 1) * 2], acc[t][e >> 1][(e & 1) * 2 + 1]}, f32x2{rs[t], rs[t]}, uu[e]);
                SB();
#pragma unroll
                for (int e = 0; e < 8; ++e) z[e] = __builtin_elementwise_fma(-uu[e], uu[e], al);
                SB();
#pragma unroll
                for (int e = 0; e < 8; ++e) z[e] *= uu[e];
                SB();
                if (t == 1) { st16c(o[0], rws, vho, sho); st16c(o[1], rws, vho, sho + 8 * 1152); }
#pragma unroll
                for (int e = 0; e < 8; ++e) z[e] = f32x2{__builtin_amdgcn_exp2f(z[e][0]), __builtin_amdgcn_exp2f(z[e][1])};
                SB();
#pragma unroll
                for (int e = 0; e < 8; ++e) z[e] += 1.f;
                SB();
#pragma unroll
                for (int e = 0; e < 8; ++e) z[e] = f32x2{__builtin_amdgcn_rcpf(z[e][0]), __builtin_amdgcn_rcpf(z[e][1])};
                SB();
#pragma unroll
                for (int e = 0; e < 8; ++e) z[e] *= uu[e];
                SB();
#pragma unroll
                for (int c = 0; c < 4; ++c) *(LDSP u32x2*)(hwp[c] + t * 16 * 144) = u32x2{pk2(z[2 * c][0], z[2 * c][1]), pk2(z[2 * c + 1][0], z[2 * c + 1][1])};
                o[2 * t] = *(LDSP u32x4*)(hrp + t * 16 * 144); o[2 * t + 1] = *(LDSP u32x4*)(hrp + (t * 16 + 8) * 144);
                SB();
            }
            st16c(o[2], rws, vho, sho + 16 * 1152); st16c(o[3], rws, vho, sho + 24 * 1152);
            if (idx < 12) bar();
            if (cb == 0 && g < 2) st4c(__float_as_uint(rbf(__builtin_amdgcn_rcpf(1.f + __builtin_amdgcn_exp2f(-1.44269504f * rbf(g ? gd : a_sum[0][1]))))), rws, l * 4, O_GATE + (rt * 128 + w * 32) * 4);
            STAMP(4);
            if (idx >= 12) {
                wait_vm<0>();
                unsigned old = 0;
                if (l == 0) old = __hip_atomic_fetch_add((LDSP unsigned*)(lds + AUXO + 2048 + 32), 1u, __ATOMIC_RELAXED, __HIP_MEMORY_SCOPE_WORKGROUP);
                if (__builtin_amdgcn_readfirstlane(old) == 3 && l < 4) st4c(tag, rws, l * 4, O_FA + b * 128);
            }
        }
    } else if (idx < 27) {
        const int tb = xc, c0 = (idx - 18) * 64;
        const int rg = l >> 3, c = l & 7;
        constexpr int TO = 8 * 9 * 1152;
        u32x4 sk[4];
        LDSP char* kb = lds + w * 9 * 1152 + c * 16;
        {
            const u32x4 rsk = mk_rsrc(shk, D * 8), rki = mk_rsrc(kin, S * D * 2), rvi = mk_rsrc(vin, S * D * 2);
#pragma unroll
            for (int i = 0; i < 4; ++i) ld16(sk[i], rsk, (c0 + c * 8) * 8, i * 16);
            const unsigned vo = (tb * 256 + w * 32 + (l >> 3)) * 1152 + c0 * 2 + (l & 7) * 16;
            dma16n(rki, w * 9 * 1152, vo - 9216, 0);
#pragma unroll
            for (int i = 0; i < 4; ++i) { dma16n(rki, (w * 9 + 1 + i) * 1152, vo, i * 9216); dma16n(rvi, (w * 9 + 5 + i) * 1152, vo, i * 9216); }
        }
        scalars();
        asm volatile("s_waitcnt vmcnt(9)" : "+v"(sk[0]), "+v"(sk[1]), "+v"(sk[2]), "+v"(sk[3]) :: "memory");
        f32x2 sf[4][4];
#pragma unroll
        for (int e = 0; e < 4; ++e) {
            sf[e][0] = f32x2{bflo(sk[e][0]), bflo(sk[e][2])}; sf[e][1] = f32x2{bfhi(sk[e][0]), bfhi(sk[e][2])};
            sf[e][2] = f32x2{bflo(sk[e][1]), bflo(sk[e][3])}; sf[e][3] = f32x2{bfhi(sk[e][1]), bfhi(sk[e][3])};
            asm volatile("" : "+v"(sf[e][0]), "+v"(sf[e][1]), "+v"(sf[e][2]), "+v"(sf[e][3]));
        }
        wait_vm<0>();
        STAMP(1);
        u32x4 kr[7], vr[4];
#pragma unroll
        for (int i = 0; i < 7; ++i) { const int j = rg + 1 + ((i + 1) >> 2); kr[i] = *(LDSP u32x4*)(kb + (j >> 1) * 1152 + ((j & 1) * 4 + ((i + 1) & 3)) * 128); }
#pragma unroll
        for (int i = 0; i < 4; ++i) vr[i] = *(LDSP u32x4*)(kb + (5 + (rg >> 1)) * 1152 + ((rg & 1) * 4 + i) * 128);
        LDSP char* tw = lds + TO + c * (8 * 512 + 16) + (63 - w * 8 - rg) * 8;
#pragma unroll
        for (int e = 0; e < 4; ++e) {
            f32x2 kk[7], u[4];
#pragma unroll
            for (int i = 0; i < 7; ++i) kk[i] = f32x2{bflo(kr[i][e]), bfhi(kr[i][e])};
#pragma unroll
            for (int i = 0; i < 4; ++i) u[i] = kk[i] * sf[e][3];
#pragma unroll
            for (int j = 1; j < 4; ++j)
#pragma unroll
                for (int i = 0; i < 4; ++i) u[i] = __builtin_elementwise_fma(kk[i + j], sf[e][3 - j], u[i]);
#pragma unroll
            for (int i = 0; i < 4; ++i) u[i] *= f32x2{bflo(vr[i][e]), bfhi(vr[i][e])};
#pragma unroll
            for (int z = 0; z < 2; ++z) *(LDSP u32x2*)(tw + (2 * e + z) * 512) = u32x2{pk2(u[3][z], u[2][z]), pk2(u[1][z], u[0][z])};
        }
        STAMP(2);
        lds_bar();
        STAMP(3);
        u32x4 uo[4];
#pragma unroll
        for (int i = 0; i < 4; ++i) { const int cl = i * 16 + (tid >> 5); uo[i] = *(LDSP u32x4*)(lds + TO + cl * 512 + (cl >> 3) * 16 + (tid & 31) * 16); }
#pragma unroll
        for (int i = 0; i < 4; ++i) st16c(uo[i], rws, (tid >> 5) * 4096 + (tid & 31) * 16, O_U + ((c0 + i * 16) * S + (1792 - tb * 256)) * 2);
        STAMP(4);
        if (w >= 4) { conv_k1(); wait_vm<4>(); } else wait_vm<0>();
        bar();
        if (tid < 4) st4c(tag, rws, tid * 4, O_FA + b * 128);
        STAMP(5);
    } else {
        scalars();
        if (w >= 4) conv_k1();
        else {
            __builtin_amdgcn_s_sleep(WSLEEP);
            warm(w2, D * D * 2, (idx - 27) * 256 + tid, 1280, 576 * 9, 1152, 9, 0);
        }
        STAMP(5);
    }
    if (idx < 12) {
        const int rt = 4 * xc + idx / 3, cb = idx % 3;
        if (w >= 4) {
            const u32x4 rh = mk_rsrc(ws + O_H, S * D * 2);
            auto issue_h = [&](int i) { dma16c(rh, HRING + (i & 3) * 8192 + u * 2048, vh[i][0], 0); dma16c(rh, HRING + (i & 3) * 8192 + u * 2048 + 1024, vh[i][1], 0); };
            if (w == 4) await(u32x4{~tag, 0u, 0u, 0u}, tag, rws, vfh);
            bar();
            issue_h(0); issue_h(1); issue_h(2); issue_h(3);
            sfor<0, 9>([&](auto I) {
                constexpr int i = I.value;
                wait_bar<g2_after(i)>();
                if constexpr (i >= 2 && i + 2 < 9) issue_h(i + 2);
                if constexpr (i == 0) issue_w2(4);
                if constexpr (i >= 2 && i + 3 < 9) issue_w2(i + 3);
            });
        } else {
            f32x4 acc[4][3];
            u32x4 qo[2][3];
#pragma unroll
            for (int c = 0; c < 3; ++c) {
                const int ct = w * 3 + c;
#pragma unroll
                for (int t = 0; t < 4; ++t) {
                    const u32x2 xv = *(LDSP u32x2*)(lds + 4 * SL + (ct >> 2) * 8192 + (t * 16 + r) * 128 + ((((ct & 3) * 2 + (g >> 1)) ^ (r >> 1)) * 16) + (g & 1) * 8);
                    acc[t][c] = f32x4{bflo(xv[0]), bfhi(xv[0]), bflo(xv[1]), bfhi(xv[1])} * GS;
                    qo[t >> 1][c][(t & 1) * 2] = xv[0]; qo[t >> 1][c][(t & 1) * 2 + 1] = xv[1];
                }
                asm volatile("" : "+v"(acc[0][c]), "+v"(acc[1][c]), "+v"(acc[2][c]), "+v"(acc[3][c]));
                if (c == 0) {
                    wait_bar<0>();
                    if (tid < 4) st4c(tag, rws, tid * 4, O_FA + b * 128);
                    STAMP(5);
                }
            }
            unsigned spo = (rt * 72 + cb * 12 + w * 3) * 1024, sso = O_STAT + (rt * 24 + cb * 4 + w) * 256;
            asm volatile("" : "+s"(spo), "+s"(sso));
#pragma unroll
            for (int rg = 0; rg < 2; ++rg)
#pragma unroll
                for (int c = 0; c < 3; ++c) st16c(qo[rg][c], rws, vl16, O_Q + spo + (rg * 36 + c) * 1024);
            LDSP char* xb[2]; LDSP char* wb[2][3];
#pragma unroll
            for (int s2 = 0; s2 < 2; ++s2) xb[s2] = opaque(lds + HRING + r * 128 + (((4 * s2 + g) ^ (r >> 1)) * 16));
#pragma unroll
            for (int hi = 0; hi < 2; ++hi)
#pragma unroll
                for (int c = 0; c < 3; ++c) { const int ct = w * 3 + c; wb[hi][c] = opaque(lds + hi * 2 * SL + (ct >> 2) * 8192 + (8 * g + q) * 128 + ((ct & 3) ^ sw) * 32 + p * 8); }
            const unsigned h1 = (r & 1) ? 0u : 0x3f803f80u, h2 = (r & 1) ? 0x3f803f80u : 0u;
            u32x4 halfu = {h1, h1, h2, h2};
            asm volatile("" : "+v"(halfu));
            bar();
            STAMP(6);
            bf16x8 xf[2][4];
            s16x4 wh[2][3][2];
            sfor<0, 19>([&](auto KS) {
                constexpr int n = KS.value, i = n >> 1, s2 = n & 1, nb = n & 1, a = nb ^ 1;
                constexpr int sl = i % 5, hi = sl >= 2, off = (sl - 2 * hi) * SL;
                if constexpr (s2 == 0 && n < 18) {
                    bar();
                    if constexpr (i == 0) STAMP(7);
                    if constexpr (i == 8) STAMP(8);
                }
                auto rd = [&](int e) {
                    if constexpr (n < 18) {
                        if (e >= 2 && e < 6) xf[nb][e - 2] = *(LDSP bf16x8*)(xb[s2] + (i & 3) * 8192 + (e - 2) * 2048);
                        else if (e < 10) { const int f = e < 2 ? e : e - 4; wh[nb][f >> 1][f & 1] = __builtin_amdgcn_ds_read_tr16_b64_v4i16((LDSP s16x4*)(wb[hi][f >> 1] + off + s2 * 4096 + (f & 1) * 512)); }
                    }
                    SB();
                };
#pragma unroll
                for (int m = 0; m < 12; ++m) {
                    if constexpr (n >= 1) {
                        mfma1<false>(acc[m & 3][m >> 2], __builtin_bit_cast(bf16x8, __builtin_shufflevector(wh[a][m >> 2][0], wh[a][m >> 2][1], 0, 1, 2, 3, 4, 5, 6, 7)), xf[a][m & 3]);
                        SB();
                    }
                    rd(m);
                }
            });
            asm volatile("" : "+v"(acc[0][0]), "+v"(acc[1][0]), "+v"(acc[2][0]), "+v"(acc[3][0]));
            STAMP(9);
            f32x4 s1[2] = {}, s2[4] = {};
#pragma unroll
            for (int c = 0; c < 3; ++c) {
                if (c) asm volatile("" : "+v"(acc[0][c]), "+v"(acc[1][c]), "+v"(acc[2][c]), "+v"(acc[3][c]));
#pragma unroll
                for (int rg = 0; rg < 2; ++rg) {
                    const f32x4 a0 = acc[2 * rg][c], a1 = acc[2 * rg + 1][c];
                    const u32x4 xo = {pk2(a0[0], a0[1]), pk2(a0[2], a0[3]), pk2(a1[0], a1[1]), pk2(a1[2], a1[3])};
                    st16c(xo, rws, vl16, O_X1 + spo + (rg * 36 + c) * 1024);
                    s1[rg] = mfma(__builtin_bit_cast(bf16x8, halfu), __builtin_bit_cast(bf16x8, xo), s1[rg]);
                    s2[2 * rg] = mfma_sq(xo[0], xo[1], s2[2 * rg]);
                    s2[2 * rg + 1] = mfma_sq(xo[2], xo[3], s2[2 * rg + 1]);
                }
            }
#pragma unroll
            for (int rg = 0; rg < 2; ++rg) {
                const u32x4 so = {__float_as_uint(s1[rg][0]), __float_as_uint(pick4(s2[2 * rg], r & 3)), __float_as_uint(s1[rg][1]), __float_as_uint(pick4(s2[2 * rg + 1], r & 3))};
                if (g == (r >> 2)) st16c(so, rws, r * 16, sso + rg * 12 * 256);
            }
            STAMP(10);
        }
    } else if (idx >= 14) {
        if (w >= 4) {
            wait_vm<0>();
#pragma unroll
            for (int c = 1; c < 4; ++c)
#pragma unroll
                for (int i = 0; i < 4; ++i) dma16(rk, cw * 16640 + c * 4160 + 64 + i * 1024, vl16 + 4 + 4 * c, i * 1024);
            if (l < 16) {
                const int c = l >> 2;
                u32x4 o = {0u, 0u, 0u, 0u};
                if ((l & 3) == 3) {
                    o[3] = c == 0 ? k0[0] : c == 1 ? k0[1] : c == 2 ? k0[2] : k0[3];
                    o[2] = c == 0 ? 0u : c == 1 ? k0[0] : c == 2 ? k0[1] : k0[2];
                    o[1] = c <= 1 ? 0u : c == 2 ? k0[0] : k0[1];
                    o[0] = c <= 2 ? 0u : k0[0];
                }
                *(LDSP u32x4*)(lds + cw * 16640 + c * 4160 + (l & 3) * 16) = o;
            }
            if (l < 56) *(LDSP u32x4*)(lds + CV_U + cw * 9152 + (l / 28) * 4576 + 4096 + (l % 28) * 16) = u32x4{0u, 0u, 0u, 0u};
        }
        if ((w >= 4) == (idx < 18)) {
            await(u32x4{~tag, 0u, 0u, 0u}, tag, rws, O_FA + ((18 + (cg >> 4)) * 8 + (l & 7)) * 128);
            STAMP(6);
            u32x4 ur[4]; unsigned nx[4];
#pragma unroll
            for (int i = 0; i < 4; ++i) ld16c(ur[i], rws, vl16, O_U + ch * 4096 + i * 1024);
            const u32x4 ru = mk_rsrc(ws + O_U + ch * 4096, 4096);
#pragma unroll
            for (int i = 0; i < 4; ++i) ld4c(nx[i], ru, vl16 + 16, i * 1024);
            asm volatile("s_waitcnt vmcnt(0)" : "+v"(ur[0]), "+v"(ur[1]), "+v"(ur[2]), "+v"(ur[3]), "+v"(nx[0]), "+v"(nx[1]), "+v"(nx[2]), "+v"(nx[3]));
            LDSP char* uw = lds + CV_U + cw * 9152 + l * 16;
#pragma unroll
            for (int i = 0; i < 4; ++i) {
                *(LDSP u32x4*)(uw + 4576 + i * 1024) = ur[i];
                *(LDSP u32x4*)(uw + i * 1024) = u32x4{__builtin_amdgcn_alignbit(ur[i][1], ur[i][0], 16), __builtin_amdgcn_alignbit(ur[i][2], ur[i][1], 16),
                                                      __builtin_amdgcn_alignbit(ur[i][3], ur[i][2], 16), __builtin_amdgcn_alignbit(nx[i], ur[i][3], 16)};
            }
        }
        wait_bar_l<0>();
        STAMP(7);
        LDSP char* yi = lds + CV_Y;
        if (w < 4) {
            LDSP char* ab = lds + cw * 16640 + (r & 3) * 4160 + ((r >> 2) + g) * 16;
            LDSP char* bb = opaque(lds + CV_U + cw * 9152 + (r & 1) * 4576 + (28 - 4 * (r >> 1) + g) * 16);
            f32x4 acc[8];
            bf16x8 A[2][8], B[2][8];
            auto ldf = [&](int t, int e) {
                if (e & 1) A[t & 1][e >> 1] = *(LDSP bf16x8*)(ab + (t + 8 * (e >> 1)) * 64);
                else B[t & 1][e >> 1] = *(LDSP bf16x8*)(bb + (7 - (e >> 1)) * 512 + t * 64);
            };
#pragma unroll
            for (int e = 0; e < 16; ++e) ldf(0, e);
            {
                const int j = 30 - 2 * r - 8 * g;
                u32x4 dv;
#pragma unroll
                for (int e = 0; e < 4; ++e) dv[e] = (j == 2 * e) ? dD : 0u;
                asm volatile("s_nop 1" : "+v"(dv));
                const bf16x8 da = __builtin_bit_cast(bf16x8, dv);
#pragma unroll
                for (int n = 0; n < 8; ++n) mfma1<true>(acc[n], da, B[0][n]);
            }
            SB();
#pragma unroll
            for (int t = 0; t < 8; ++t) {
                int cnt = 0;
#pragma unroll
                for (int m = 0; m < 8; ++m)
#pragma unroll
                    for (int n = 0; n + m < 8; ++n) {
                        mfma1<false>(acc[m + n], A[t & 1][m], B[t & 1][n]);
                        if (t < 7 && (cnt & 1) == 0 && cnt < 32) ldf(t + 1, cnt >> 1);
                        ++cnt;
                        SB();
                    }
            }
            asm volatile("s_nop 7\n\ts_nop 1" : "+v"(acc[0]), "+v"(acc[1]), "+v"(acc[2]), "+v"(acc[3]), "+v"(acc[4]), "+v"(acc[5]), "+v"(acc[6]), "+v"(acc[7]));
            STAMP(9);
            LDSP char* yw = opaque(yi + (r >> 1) * 272 + (8 * g + (r & 1)) * 8 + cw * 2);
#pragma unroll
            for (int c = 0; c < 8; ++c)
#pragma unroll
                for (int e = 0; e < 4; e += 2) {
                    const unsigned o = pk2(acc[c][e], acc[c][e + 1]);
                    *(LDSP u16*)(yw + c * 2176 + e * 16) = (u16)o;
                    *(LDSP u16*)(yw + c * 2176 + e * 16 + 16) = (u16)(o >> 16);
                }
        }
        else {
            if (idx >= 18) {
                __builtin_amdgcn_s_sleep(W3SLEEP);
                warm(w3, D * F * 2, (idx - 18) * 256 + tid - 256, 14 * 256, 576 * 18, 9216, 18, (xc & 3) * 2304);
            }
        }
        lds_bar();
#pragma unroll
        for (int i = 0; i < 2; ++i) {
            const int r32 = i * 32 + (tid >> 4);
            const u32x2 lo = *(LDSP u32x2*)(yi + r32 * 272 + r * 8), hi = *(LDSP u32x2*)(yi + r32 * 272 + 128 + r * 8);
            st16c(u32x4{lo[0], lo[1], hi[0], hi[1]}, rws, (tid >> 4) * 36864 + r * 16, O_Y + (i * 32 * 36 + (cg >> 2)) * 1024 + (cg & 3) * 256);
        }
        STAMP(10);
    } else {
    }
    DUMP(0, 12); }
    const int rt = rt3;
    const int wi = w >> 2, wj = w & 3;
    const int rho = rt % 9;
    DECL;
    const unsigned pbase = O_X1 + ((rt * 2 + wi) * 36 + wj) * 1024;
    u32x4 sr[3], xr[9];
    const u32x4 r3 = mk_rsrc((const char*)w3 + fc * 1152 + (size_t)w * 8 * 9216, D * F * 2 - (fc * 1152 + w * 8 * 9216));
    const u32x4 r4 = mk_rsrc((const char*)w4 + (size_t)(fc * 576 + w * 8) * 1152, D * F * 2 - (fc * 576 + w * 8) * 1152);
    const unsigned vsw = ((l & 7) ^ (g << 1)) * 16;
    const unsigned vo3 = (l >> 3) * 9216 + vsw, vo4 = (l >> 3) * 1152 + vsw;
    const unsigned ldsw = w * 1024;
    auto sigma = [&](int pos) { return pos == 0 ? 8 : ((fc + pos) & 7); };
    auto issue = [&](auto N) {
        constexpr int n = N.value, st = n / 9, kk = n % 9;
        if constexpr (st < 9) dma16(r3, ldsw + (st & 1) * HALF + kk * 8192, vo3, kk * 64 * 9216 + wrap9(st + rho) * 128);
        else dma16(r4, ldsw + (st & 1) * HALF + kk * 8192, vo4, wrap9(kk + rho) * 73728 + wrap9(sigma(st - 9) + rho) * 128);
    };
    lds_bar();
    sfor<0, 3>(issue);
    wait_bar<3>();
    asm volatile("" :: "v"(sink));
    if (tid < 4) st4c(tag, rws, tid * 4, O_FB + b * 128);
    sfor<3, 9>(issue);
    unsigned vyf;
    {
        const int c2 = l < 16 ? wrap9(fc + rho) * 16 + l : wrap9(8 + rho) * 16 + (fc & 3) * 4 + (l & 3);
        vyf = O_FB + ((14 + c2 % 18) * 8 + c2 / 18) * 128;
        pin(vyf);
    }
    if (w == 0) await(u32x4{~tag, 0u, 0u, 0u}, tag, rws, O_FB + (((rt & 3) * 3 + min(l, 2)) * 8 + (rt >> 2)) * 128);
    bar();
    STAMP(0);
#pragma unroll
    for (int i = 0; i < 3; ++i) ld16c(sr[i], rws, vl16, O_STAT + (rt * 2 + wi) * 3072 + i * 1024);
#pragma unroll
    for (int i = 0; i < 9; ++i) {
        if (i == 4) bar();
        ld16c(xr[i], rws, vl16, pbase + i * 4096);
    }
    LDSP char* gr = lds + wi * 4096 + l * 16;
    LDSP char* gw = lds + wi * 4096 + (wj >> 1) * 1024 + l * 16 + (wj & 1) * 8;
    float sc[2], of[2];
    asm volatile("s_waitcnt vmcnt(9)" : "+v"(sr[0]), "+v"(sr[1]), "+v"(sr[2]));
#pragma unroll
    for (int t = 0; t < 2; ++t) {
        const float s1 = sum_g(__uint_as_float(sr[0][2 * t]) + __uint_as_float(sr[1][2 * t]) + __uint_as_float(sr[2][2 * t]));
        const float s2 = sum_g(__uint_as_float(sr[0][2 * t + 1]) + __uint_as_float(sr[1][2 * t + 1]) + __uint_as_float(sr[2][2 * t + 1]));
        const float mean = s1 * (1.f / D);
        const float var = fmaxf(s2 * (1.f / D) - mean * mean, 0.f);
        sc[t] = GS * __builtin_amdgcn_rsqf(var + 1e-5f * GS * GS); of[t] = -mean * sc[t];
    }
    STAMP(1);
    LDSP char* xw = opaque(gw + HALF + 8192);
    auto putx = [&](auto I) {
        constexpr int i = I.value;
        u32x4& v = xr[i];
        asm volatile("s_waitcnt vmcnt(%1)" : "+v"(v) : "n"(8 - i));
#pragma unroll
        for (int t = 0; t < 2; ++t)
            *(LDSP u32x2*)((i == 0 ? gw + HALF : xw + (i - 1) * 8192) + t * 2048) = u32x2{norm2(v[2 * t], sc[t], of[t]), norm2(v[2 * t + 1], sc[t], of[t])};
    };
    sfor<0, 4>(putx);
    STAMP(2);
    bf16x8 X[36];
    LDSP char* tb = lds + (4 * g + q) * 128 + (wj ^ sw) * 32 + p * 8;
    LDSP char* tbs[3] = {tb, opaque(tb + 65536), opaque(tb + 131072)};
    LDSP char* grs[3] = {gr, opaque(gr + 65536), opaque(gr + 131072)};
    auto ldw = [&](auto KS) -> bf16x8 {
        constexpr int st = KS.value / 18, ks = KS.value % 18, off = (st & 1) * HALF + ks * 4096;
        return ld_tr(tbs[off >> 16] + (off & 65535), 2048);
    };
    auto ldx = [&](auto KS) {
        constexpr int gk = KS.value, ks = gk % 18;
        if constexpr (gk < 18 || (gk >= 162 && gk < 180)) {
            constexpr int off = (gk < 18 ? HALF : 0) + (ks >> 1) * 8192 + (ks & 1) * 1024;
            LDSP char* a = grs[off >> 16] + (off & 65535);
            X[2 * ks] = *(LDSP bf16x8*)a; X[2 * ks + 1] = *(LDSP bf16x8*)(a + 2048);
        }
    };
    bf16x8 Wf[3];
    unsigned Gown[9][4];
    f32x4 acc[2][2];
    GeluT T;
    auto putg = [&](int s, int t) { *(LDSP u32x2*)(gw + s * 8192 + t * 2048) = u32x2{Gown[s][2 * t], Gown[s][2 * t + 1]}; };
    const unsigned pt = O_PART + rt * 9 * 8 * 8 * 1024 + w * 1024;
    auto mine = [&](int pos) { return pt + (unsigned)((sigma(pos) * 8 + fc) * 8192); };
    auto theirs = [&](int pos) { return pt + (unsigned)((fc * 8 + ((fc - pos) & 7)) * 8192); };
    Side sd, se;
    u32x4 pv[8], ev[8], sel[2];
    u32x4 fl;
    unsigned gt[2];
    float base[8];

    sfor<0, 18>([&](auto ST) {
        constexpr int st = ST.value;
        sfor<0, 18>([&](auto KS) {
            constexpr int ks = KS.value, gk = st * 18 + ks, G = gk / 6, k = gk % 6;
            if constexpr (k == 0 && G <= 51) {
                static_assert(SCH.vm[G] >= 0 && SCH.vm[G] < 64, "schedule");
                if constexpr (G == G_YCHK || G == G_CHK) {
                    if (w == 0) {
                        u32x4 v = fl;
                        asm volatile("s_waitcnt vmcnt(%1)" : "+v"(v) : "n"(G == G_CHK ? SCH.pollwait : SCH.ywait));
                        await(v, tag, rws, vyf);
                    }
                    if constexpr (G == G_YCHK) { vyf = O_FP + (((rt & 15) * 2 + (l & 1)) * 8 + (rt >> 4) * 4 + ((l >> 1) & 3)) * 128; pin(vyf); }
                }
                if constexpr (G <= 2 || (G >= 26 && G <= 28)) wait_bar_l<SCH.vm[G]>(); else wait_bar<SCH.vm[G]>();
                if constexpr (G == G_PUB) { if (tid < 4) st4c(tag, rws, tid * 4, O_FP + b * 128); }
                if constexpr (G == G_YPOLL || G == G_POLL) ld_flag(fl, w, rws, vyf);
                if constexpr (gk == 0) { ldx(IC(0)); Wf[0] = ldw(IC(0)); ldx(IC(1)); Wf[1] = ldw(IC(1)); }
            }
            if constexpr (ks == 0) STAMP(3 + st);
            SB();
            f32x4* A = acc[st & 1];
            f32x4* P = acc[(st & 1) ^ 1];
            constexpr bool late_reads = st >= 1 && G < 25;
            auto reads = [&]() { if constexpr (gk + 2 < 324) { Wf[(gk + 2) % 3] = ldw(IC(gk + 2)); ldx(IC(gk + 2)); } SB(); };
            auto dma = [&]() { if constexpr (SCH.dma[G][k] >= 0) issue(IC(SCH.dma[G][k] >= 0 ? SCH.dma[G][k] : 0)); SB(); };
            const bf16x8 Wk = Wf[gk % 3];
            mfma1<ks == 0>(A[0], Wk, X[2 * ks]);
            SB();
            if constexpr (late_reads) dma(); else reads();
            if constexpr (G == 25 || G == 26) { putg(3 * (G - 25) + k / 2, k & 1); SB(); }
            if constexpr (G == 27) { putg(6 + k / 2, k & 1); SB(); }
            mfma1<ks == 0>(A[1], Wk, X[2 * ks + 1]);
            SB();
            if constexpr (late_reads) reads(); else dma();
            if constexpr (st >= 1 && st <= 8) {
                if constexpr ((ks & 1) == 0) gelu_even<ks / 2>(T, P);
                else gelu_odd<ks / 2>(T, P, Gown[st - 1]);
            }
            if constexpr (G == 0 && (k & 1)) putx(IC(4 + k / 2));
            if constexpr (G == 1 && k < 2) putx(IC(7 + k));
            if constexpr (G == 27 && k < 4) Gown[8][k] = gelu2(P[k >> 1][(k & 1) * 2], P[k >> 1][(k & 1) * 2 + 1]);
            if constexpr (n_store(G, k)) {
                u32x4 o = {pk2(P[0][0], P[0][1]), pk2(P[0][2], P[0][3]), pk2(P[1][0], P[1][1]), pk2(P[1][2], P[1][3])};
                if constexpr (st >= 14) {
#pragma unroll
                    for (int e = 0; e < 4; ++e) o[e] = min(o[e], 0xfffffffeu);
                }
                st16c(o, rws, vl16, mine(st - 10));
            }
            if constexpr (fill_at(G, k) >= 0) st16c(u32x4{~0u, ~0u, ~0u, ~0u}, rws, vl16, mine(fill_at(G, k)));
            if constexpr (G == 27) {
                const unsigned po = pbase + wrap9(fc + rho) * 4096;
                if constexpr (k == 0) ld16c(sd.x, rws, vl16, po);
                if constexpr (k == 1) ld16c(sd.gq, rws, vl16, po + NB);
                if constexpr (k == 2) ld16c(sd.y, rws, vl16, po + 2 * NB);
                if constexpr (k == 3) ld4c(gt[0], rws, r * 4, O_GATE + (rt * 64 + wi * 32) * 4);
                if constexpr (k == 4) ld4c(gt[1], rws, r * 4, O_GATE + (rt * 64 + wi * 32 + 16) * 4);
            }
            if constexpr ((G == 34 || G == 35) && (k == 2 || k == 5)) {
                constexpr int e = (G - 34) * 2 + (k == 5);
                mix_base(base[2 * e], base[2 * e + 1], sd.gq[e], sd.y[e], __uint_as_float(gt[e >> 1]));
            }
            if constexpr (get_at(G, k) >= 0) ld16c(pv[get_at(G, k) >= 0 ? get_at(G, k) : 0], rws, vl16, theirs(get_at(G, k)));
            if constexpr (G == 52 && k == 1) ld_shared(ev, se, w - fc, rws, vl16, pt + 64 * 8192, pbase + wrap9(8 + rho) * 4096);
            if constexpr (G == 51 && k == 2) {
                const unsigned one = (r >> 2) == g ? ((r & 1) ? 0x3f800000u : 0x3f80u) : 0u;
                sel[0] = u32x4{(r & 2) ? 0u : one, (r & 2) ? one : 0u, 0u, 0u};
                sel[1] = u32x4{0u, 0u, sel[0][0], sel[0][1]};
                u32x4* ss = sel;
                asm volatile("" : "+v"(ss[0]), "+v"(ss[1]));
            }
            if constexpr (G == 52 && k == 0) add_piece(A[0], A[1], sel[0], sel[1], sd.x);
            if constexpr (G == 53 && k == 0) wait_vm<SCH.endwait>();
            if constexpr (G == 53 && k < 3) add_piece(A[0], A[1], sel[0], sel[1], pv[k + 1]);
            SB();
        });
    });
    STAMP(21);
    LDSP char* scr = lds + 2 * HALF + w * 2048;
    auto ro = __builtin_amdgcn_make_buffer_rsrc((void*)(out + (size_t)(rt * 64 + wi * 32) * D + wj * 16), 0, 32 * D * 2 - wj * 32, 0x00020000);
    const unsigned vout = (l >> 1) * (D * 2) + (l & 1) * 16;
    u32x4 ve;
    asm volatile("" : "+v"(gt[0]), "+v"(gt[1]));
    const float ngs[2] = {rbf(1.f - __uint_as_float(gt[0])) * GINV, rbf(1.f - __uint_as_float(gt[1])) * GINV};
    if (w == fc) {
        f32x4 c[2] = {};
        float be[8];
        asm volatile("s_waitcnt vmcnt(1)\n\ts_nop 1" : "+v"(c[0]), "+v"(c[1]), "+v"(ev[0]), "+v"(ev[1]), "+v"(ev[2]), "+v"(ev[3]), "+v"(ev[4]), "+v"(ev[5]), "+v"(ev[6]), "+v"(ev[7]));
#pragma unroll
        for (int i = 0; i < 8; ++i) add_piece(c[0], c[1], sel[0], sel[1], ev[i]);
        add_piece(c[0], c[1], sel[0], sel[1], se.x);
#pragma unroll
        for (int e = 0; e < 4; ++e) mix_base(be[2 * e], be[2 * e + 1], se.gq[e], se.y[e], __uint_as_float(gt[e >> 1]));
        asm volatile("" : "+v"(c[0]), "+v"(c[1]));
        ve = mix_out(be, ngs, c, scr, l);
    }
    {
        asm volatile("s_waitcnt vmcnt(0)" : "+v"(pv[4]), "+v"(pv[5]), "+v"(pv[6]), "+v"(pv[7]));
        if (w == fc) __builtin_amdgcn_raw_buffer_store_b128(ve, ro, vout, wrap9(8 + rho) * 128, OAUX);
        const unsigned so[4] = {theirs(4), theirs(5), theirs(6), theirs(7)};
        settle<4>(pv + 4, so, rws, vl16);
        STAMP(22);
#pragma unroll
        for (int i = 4; i < 8; ++i) add_piece(acc[1][0], acc[1][1], sel[0], sel[1], pv[i]);
        asm volatile("s_nop 7\n\ts_nop 1" : "+v"(acc[1][0]), "+v"(acc[1][1]));
        __builtin_amdgcn_raw_buffer_store_b128(mix_out(base, ngs, acc[1], scr, l), ro, vout, wrap9(fc + rho) * 128, OAUX);
    }
    if (b == 0 && w < 4) {
        await(u32x4{~tag, 0u, 0u, 0u}, tag, rws, O_FB + tid * 128);
    }
    if (b == 0) {
        bar();
        if (tid == 0) st4c(tag, rws, 0, 0);
    }
    asm volatile("k_all_code_hi:");
    DRAIN(); STAMP(23); DUMP(16, 24);
}

extern "C" void kernel_launch(void* const* d_in, const int* in_sizes, int n_in, void* d_out, int out_size, void* d_ws, size_t ws_size, hipStream_t stream) {
    (void)hipFuncSetAttribute((const void*)k_all, hipFuncAttributeMaxDynamicSharedMemorySize, 163840);
    k_all<<<256, 512, 163840, stream>>>((const u16*)d_in[0], (const u16*)d_in[1], (const u16*)d_in[2], (const u16*)d_in[3], (const u16*)d_in[4], (const u16*)d_in[5],
                                        (const u16*)d_in[7], (const u16*)d_in[8], (const u16*)d_in[9], (const u16*)d_in[10], (const u16*)d_in[11],
                                        (char*)d_ws, (u16*)d_out);
}
```

```cpp
#include <hip/hip_runtime.h>
#include <hip/hip_bf16.h>
#include <stdint.h>
#include <type_traits>

typedef unsigned short u16;
typedef __attribute__((ext_vector_type(4))) short s16x4;
typedef __attribute__((ext_vector_type(8))) __bf16 bf16x8;
typedef __attribute__((ext_vector_type(2))) __bf16 bf16x2;
typedef __attribute__((ext_vector_type(4))) float f32x4;
typedef __attribute__((ext_vector_type(2))) float f32x2;
typedef __attribute__((ext_vector_type(4))) unsigned u32x4;
typedef __attribute__((ext_vector_type(2))) unsigned u32x2;
#define LDSP __attribute__((address_space(3)))
#define DI __device__ __forceinline__
#define SB() __builtin_amdgcn_sched_barrier(0)
#define IC(v) std::integral_constant<int, (v)>{}

constexpr int S = 2048, D = 576, F = 4608;
constexpr float GS = 0.46866869f;
constexpr float GALPHA = -4.91222954f;
constexpr float GINV = 2.13370343f;

#define WSLEEP 40
#define WREG 2
#define MAXPG1 6
#define MAXPG2 6

template <int I, int N, class Fn> DI void sfor(Fn&& f) {
    if constexpr (I < N) { f(std::integral_constant<int, I>{}); sfor<I + 1, N>(f); }
}
DI float bflo(unsigned v) { return __uint_as_float(v << 16); }
DI float bfhi(unsigned v) { return __uint_as_float(v & 0xffff0000u); }
DI unsigned pk2(float a, float b) { f32x2 v = {a, b}; return __builtin_bit_cast(unsigned, __builtin_convertvector(v, bf16x2)); }
DI float rbf(float f) { return bflo(pk2(f, 0.f)); }
DI int wrap9(int v) { return v >= 9 ? v - 9 : v; }
DI u32x4 mk_rsrc(const void* p, unsigned bytes) {
    unsigned long long a = (unsigned long long)p;
    u32x4 r = {(unsigned)a, (unsigned)(a >> 32) & 0xffffu, bytes, 0x00020000u};
    return r;
}
DI void dma16(u32x4 rs, unsigned lds, unsigned voff, unsigned soff) {
    asm volatile("s_mov_b32 m0, %0\n\ts_nop 0\n\tbuffer_load_dwordx4 %1, %2, %3 offen lds" :: "s"(lds), "v"(voff), "s"(rs), "s"(soff) : "memory");
}
DI void dma16c(u32x4 rs, unsigned lds, unsigned voff, unsigned soff) {
    asm volatile("s_mov_b32 m0, %0\n\ts_nop 0\n\tbuffer_load_dwordx4 %1, %2, %3 offen sc1 lds" :: "s"(lds), "v"(voff), "s"(rs), "s"(soff) : "memory");
}
DI void dma16n(u32x4 rs, unsigned lds, unsigned voff, unsigned soff) {
    asm volatile("s_mov_b32 m0, %0\n\ts_nop 0\n\tbuffer_load_dwordx4 %1, %2, %3 offen nt lds" :: "s"(lds), "v"(voff), "s"(rs), "s"(soff) : "memory");
}
DI void ld16(u32x4& d, u32x4 rs, unsigned vo, unsigned so) { asm volatile("buffer_load_dwordx4 %0, %1, %2, %3 offen" : "=v"(d) : "v"(vo), "s"(rs), "s"(so)); }
DI void ld16c(u32x4& d, u32x4 rs, unsigned vo, unsigned so) { asm volatile("buffer_load_dwordx4 %0, %1, %2, %3 offen sc1" : "=v"(d) : "v"(vo), "s"(rs), "s"(so)); }
DI void ld4c(unsigned& d, u32x4 rs, unsigned vo, unsigned so) { asm volatile("buffer_load_dword %0, %1, %2, %3 offen sc1" : "=v"(d) : "v"(vo), "s"(rs), "s"(so)); }
DI void ld2c(unsigned& d, u32x4 rs, unsigned vo, unsigned so) { asm volatile("buffer_load_ushort %0, %1, %2, %3 offen sc1" : "=v"(d) : "v"(vo), "s"(rs), "s"(so)); }
DI void st16c(u32x4 v, u32x4 rs, unsigned vo, unsigned so) { asm volatile("buffer_store_dwordx4 %0, %1, %2, %3 offen sc1\n\ts_nop 1" :: "v"(v), "v"(vo), "s"(rs), "s"(so) : "memory"); }
DI void st8c(u32x2 v, u32x4 rs, unsigned vo, unsigned so) { asm volatile("buffer_store_dwordx2 %0, %1, %2, %3 offen sc1" :: "v"(v), "v"(vo), "s"(rs), "s"(so) : "memory"); }
DI void st4c(unsigned v, u32x4 rs, unsigned vo, unsigned so) { asm volatile("buffer_store_dword %0, %1, %2, %3 offen sc1" :: "v"(v), "v"(vo), "s"(rs), "s"(so) : "memory"); }
DI void st2c(unsigned v, u32x4 rs, unsigned vo, unsigned so) { asm volatile("buffer_store_short %0, %1, %2, %3 offen sc1" :: "v"(v), "v"(vo), "s"(rs), "s"(so) : "memory"); }
template <int N> DI void wait_vm() { asm volatile("s_waitcnt vmcnt(%0)" :: "n"(N) : "memory"); }
DI void bar() { asm volatile("s_barrier" ::: "memory"); }
template <int N> DI void wait_bar() { asm volatile("s_waitcnt vmcnt(%0)\n\ts_barrier" :: "n"(N) : "memory"); }
template <int N> DI void wait_bar_l() { asm volatile("s_waitcnt vmcnt(%0) lgkmcnt(0)\n\ts_barrier" :: "n"(N) : "memory"); }
DI void lds_bar() { asm volatile("s_waitcnt lgkmcnt(0)\n\ts_barrier" ::: "memory"); }
DI s16x4 tr4(LDSP char* a) { return __builtin_amdgcn_ds_read_tr16_b64_v4i16((LDSP s16x4*)a); }
DI bf16x8 join(s16x4 lo, s16x4 hi) { return __builtin_bit_cast(bf16x8, __builtin_shufflevector(lo, hi, 0, 1, 2, 3, 4, 5, 6, 7)); }
DI bf16x8 ld_tr(LDSP char* a, int second) { const s16x4 lo = tr4(a), hi = tr4(a + second); return join(lo, hi); }
DI void pin(unsigned& v) { asm volatile("" : "+v"(v)); }
DI LDSP char* opaque(LDSP char* p) { asm volatile("" : "+v"(p)); return p; }
DI f32x4 mfma(bf16x8 a, bf16x8 b, f32x4 c) { return __builtin_amdgcn_mfma_f32_16x16x32_bf16(a, b, c, 0, 0, 0); }
template <bool FIRST> DI void mfma1(f32x4& c0, const bf16x8& a, const bf16x8& b0) {
    if constexpr (FIRST) asm volatile("v_mfma_f32_16x16x32_bf16 %0, %1, %2, 0" : "=&v"(c0) : "v"(a), "v"(b0));
    else asm volatile("v_mfma_f32_16x16x32_bf16 %0, %1, %2, %0" : "+v"(c0) : "v"(a), "v"(b0));
}
DI float sum_g(float v) {
    const auto a = __builtin_amdgcn_permlane16_swap(__float_as_uint(v), __float_as_uint(v), false, false);
    v = __uint_as_float(a[0]) + __uint_as_float(a[1]);
    const auto c = __builtin_amdgcn_permlane32_swap(__float_as_uint(v), __float_as_uint(v), false, false);
    return __uint_as_float(c[0]) + __uint_as_float(c[1]);
}
DI float pick4(const f32x4& v, int i) { return i == 0 ? v[0] : i == 1 ? v[1] : i == 2 ? v[2] : v[3]; }
DI unsigned umax4(const u32x4& v) { return max(max(v[0], v[1]), max(v[2], v[3])); }
#define STAMP(i) do {} while (0)
#define DUMP(lo, n) do {} while (0)
#define DECL do {} while (0)
#define DRAIN() do {} while (0)

struct GeluT { float t0, t2, u[8]; };
template <int e> DI void gelu_blk(GeluT& T, const f32x4* P, const float* rs, unsigned* Go) {
    float t1;
    if constexpr (e == 0)
        asm volatile("v_mul_f32 %1, %3, %5\n\tv_mul_f32 %2, %4, %5\n\tv_fma_f32 %0, -%1, %1, %6\n\ts_nop 0\n\tv_mul_f32 %0, %0, %1\n\ts_nop 0\n\tv_exp_f32 %0, %0"
                     : "=&v"(T.t0), "=&v"(T.u[0]), "=&v"(T.u[1]) : "v"(P[0][0]), "v"(P[0][1]), "v"(rs[0]), "s"(GALPHA));
    else if constexpr (e < 7 && (e & 1))
        asm volatile("v_add_f32 %1, 1.0, %0\n\tv_fma_f32 %0, -%3, %3, %7\n\tv_rcp_f32 %1, %1\n\tv_mul_f32 %0, %0, %3\n\tv_mul_f32 %2, %5, %6\n\tv_exp_f32 %0, %0\n\tv_mul_f32 %1, %1, %4"
                     : "+v"(T.t0), "=&v"(T.t2), "=&v"(T.u[e + 1]) : "v"(T.u[e]), "v"(T.u[e - 1]), "v"(P[(e + 1) >> 2][(e + 1) & 3]), "v"(rs[(e + 1) >> 2]), "s"(GALPHA));
    else if constexpr (e < 7)
        asm volatile("v_add_f32 %1, 1.0, %0\n\tv_fma_f32 %0, -%4, %4, %8\n\tv_rcp_f32 %1, %1\n\tv_mul_f32 %0, %0, %4\n\tv_mul_f32 %2, %6, %7\n\tv_mul_f32 %1, %1, %5\n\tv_exp_f32 %0, %0\n\t"
                     "v_cvt_pk_bf16_f32 %3, %9, %1"
                     : "+v"(T.t0), "=&v"(t1), "=&v"(T.u[e + 1]), "=v"(Go[(e - 1) >> 1])
                     : "v"(T.u[e]), "v"(T.u[e - 1]), "v"(P[(e + 1) >> 2][(e + 1) & 3]), "v"(rs[(e + 1) >> 2]), "s"(GALPHA), "v"(T.t2));
    else if constexpr (e == 7)
        asm volatile("v_add_f32 %1, 1.0, %0\n\tv_fma_f32 %0, -%2, %2, %4\n\tv_rcp_f32 %1, %1\n\tv_mul_f32 %0, %0, %2\n\tv_mul_f32 %1, %1, %3\n\tv_exp_f32 %0, %0"
                     : "+v"(T.t0), "=&v"(T.t2) : "v"(T.u[7]), "v"(T.u[6]), "s"(GALPHA));
    else
        asm volatile("v_add_f32 %0, 1.0, %2\n\ts_nop 0\n\tv_rcp_f32 %0, %0\n\ts_nop 0\n\tv_mul_f32 %0, %0, %3\n\ts_nop 0\n\tv_cvt_pk_bf16_f32 %1, %4, %0"
                     : "=&v"(t1), "=v"(Go[3]) : "v"(T.t0), "v"(T.u[7]), "v"(T.t2));
}
DI unsigned gelu2(float v0, float v1, float rs) {
    float a, b, u0, u1; unsigned o;
    asm volatile("v_mul_f32 %3, %5, %7\n\tv_mul_f32 %4, %6, %7\n\t"
                 "v_fma_f32 %0, -%3, %3, %8\n\tv_fma_f32 %1, -%4, %4, %8\n\tv_mul_f32 %0, %0, %3\n\tv_mul_f32 %1, %1, %4\n\tv_exp_f32 %0, %0\n\tv_exp_f32 %1, %1\n\t"
                 "v_add_f32 %0, 1.0, %0\n\tv_add_f32 %1, 1.0, %1\n\tv_rcp_f32 %0, %0\n\tv_rcp_f32 %1, %1\n\tv_mul_f32 %0, %0, %3\n\tv_mul_f32 %1, %1, %4\n\ts_nop 0\n\t"
                 "v_cvt_pk_bf16_f32 %2, %0, %1" : "=&v"(a), "=&v"(b), "=v"(o), "=&v"(u0), "=&v"(u1) : "v"(v0), "v"(v1), "v"(rs), "s"(GALPHA));
    return o;
}

constexpr int HALF = 73728;
#define XPRE 6
#define GET7 161
#define W4N 5120
#define NB3 99
#define XNT 1
#define TIT 0
#define TNT ""
#define NL 156
#define FILLS 16, 25, 34, 43, 52
#define NCHK 129
#define NSH 156
#define W4T 2
#define T0 18
#define TS 3
#define B1 5
#define B2 3
#define L1 13
#define L2 9
constexpr bool is_bar(int n) {
    return n <= 15 ? n % 3 == 0 : n <= 78 ? (n > 17 && (78 - n) % B1 == 0) : n <= NB3 ? n % 3 == 0 : n <= NL ? (n > NB3 + 2 && (NL - n) % B2 == 0) : false;
}
constexpr int bar_from(int n) { while (!is_bar(n)) ++n; return n; }
#define GETS 130, 134, 138, 142, 146, 154, 160
#define NSIDE 81
#define NADD 155
constexpr int N_SIDE = bar_from(NSIDE), N_YCHK = bar_from(62), N_YPOLL = N_YCHK - 7, N_PUB = bar_from(113), N_CHK = bar_from(NCHK), N_POLL = N_CHK - 5, N_ADD = NADD;
constexpr int fill_at(int n) { constexpr int t[5] = {FILLS}; for (int i = 0; i < 5; ++i) if (t[i] == n) return 3 + i; return -1; }
constexpr int get_n(int i) { constexpr int t[7] = {GETS}; return t[i - 1]; }
constexpr int n_get(int n) { int c = 0; for (int i = 1; i < 8; ++i) c += get_n(i) == n; return c; }
constexpr int n_store(int n) { return n >= 90 && n % 9 == 0; }
constexpr int n_side(int n) { return n == N_SIDE ? 2 : n == N_SIDE + 1 ? 2 : n == N_SIDE + 2 ? 2 : 0; }
struct Sched { int d1[162], d2[162], vm[162]; int ywait, pollwait, endwait; };
constexpr Sched make_sched() {
    Sched s{};
    int next = 9, a = 0;
    for (int n = 0; n < 162; ++n) {
        if (is_bar(n)) a = n;
        s.d1[n] = s.d2[n] = -1;
        for (int c = 0; c < 2; ++c) {
            const int m = next;
            const int freed = m < 18 ? m - 9 : (m >= 90 && m < 99) ? m - 9 : m - 18;
            if (m > 161 || a <= freed || (n >= 78 && n < 84) || m - n > (n < 81 ? L1 : L2)) break;
            if (c == 1 && n < 81 && (n & 1)) break;
            (c ? s.d2[n] : s.d1[n]) = m; ++next;
        }
    }
    int pos_dma[162] = {}, before[163] = {}, pos_st3 = 0, pos_get3 = 0;
    int p = 0;
    for (int n = 0; n < 9; ++n) pos_dma[n] = p++;
    p += XPRE;
    for (int n = 0; n < 162; ++n) {
        before[n] = p;
        if (n == 0) p += 12 - XPRE;
        if (s.d1[n] >= 0) pos_dma[s.d1[n]] = p++;
        if (s.d2[n] >= 0) pos_dma[s.d2[n]] = p++;
        if (n == 108) pos_st3 = p;
        p += n_store(n);
        p += (fill_at(n) >= 0) + n_get(n) + n_side(n);
        if (get_n(5) == n) pos_get3 = p - 1;
    }
    for (int n = 0; n < 162; ++n) {
        s.vm[n] = -1;
        if (!is_bar(n)) continue;
        int last = pos_dma[n == NL ? 161 : bar_from(n + 1)];
        if (n < 6) last = 9 + n + 3;
        if (n == N_PUB && pos_st3 > last) last = pos_st3;
        if (last > 0 && last < before[n]) s.vm[n] = before[n] - 1 - last;
    }
    s.ywait = before[N_YCHK] - before[N_YPOLL];
    s.pollwait = before[N_CHK] - before[N_POLL];
    s.endwait = before[N_ADD] - 1 - pos_get3;
    return s;
}
constexpr Sched SCH = make_sched();

struct Side { u32x4 x[2], q; };
DI void mix_base(float& b0, float& b1, unsigned qq, unsigned y0, unsigned y1, float gv, float c) {
    float t0, t1;
    asm volatile("v_lshlrev_b32 %0, 16, %4\n\tv_and_b32 %1, 0xffff0000, %4\n\tv_lshlrev_b32 %2, 16, %5\n\tv_lshlrev_b32 %3, 16, %6\n\t"
                 "v_mul_f32 %0, %0, %2\n\tv_mul_f32 %1, %1, %3\n\tv_fma_f32 %0, %0, %7, %8\n\tv_fma_f32 %1, %1, %7, %8"
                 : "=&v"(b0), "=&v"(b1), "=&v"(t0), "=&v"(t1) : "v"(qq), "v"(y0), "v"(y1), "v"(gv), "v"(c));
}
DI void add2(f32x4& c0, f32x4& c1, const u32x4& s0, const u32x4& s1, const u32x4& v0, const u32x4& v1) {
    asm volatile("v_mfma_f32_16x16x32_bf16 %0, %2, %4, %0\n\tv_mfma_f32_16x16x32_bf16 %1, %3, %5, %1" : "+v"(c0), "+v"(c1) : "v"(s0), "v"(s1), "v"(v0), "v"(v1));
}
DI void settle(u32x4& v, unsigned so, u32x4 rp, unsigned vo) {
#pragma unroll 1
    for (int it = 0; it < (1 << 17); ++it) {
        if (__builtin_amdgcn_ballot_w64(umax4(v) == 0xffffffffu) == 0) break;
        asm volatile("buffer_load_dwordx4 %0, %1, %2, %3 offen sc1\n\ts_waitcnt vmcnt(0)" : "=&v"(v) : "v"(vo), "s"(rp), "s"(so) : "memory");
    }
}
DI void ld_shared(u32x4* ev, Side& se, int skip, u32x4 rs, unsigned vo, unsigned so, unsigned sx, unsigned sq) {
    unsigned t;
    asm volatile("s_cmp_lg_u32 %12, 0\n\ts_cbranch_scc1 1f\n\t"
                 "buffer_load_dwordx4 %0, %14, %13, %15 offen sc1\n\ts_add_u32 %11, %15, 0x2000\n\t"
                 "buffer_load_dwordx4 %1, %14, %13, %11 offen sc1\n\ts_add_u32 %11, %15, 0x4000\n\t"
                 "buffer_load_dwordx4 %2, %14, %13, %11 offen sc1\n\ts_add_u32 %11, %15, 0x6000\n\t"
                 "buffer_load_dwordx4 %3, %14, %13, %11 offen sc1\n\ts_add_u32 %11, %15, 0x8000\n\t"
                 "buffer_load_dwordx4 %4, %14, %13, %11 offen sc1\n\ts_add_u32 %11, %15, 0xa000\n\t"
                 "buffer_load_dwordx4 %5, %14, %13, %11 offen sc1\n\ts_add_u32 %11, %15, 0xc000\n\t"
                 "buffer_load_dwordx4 %6, %14, %13, %11 offen sc1\n\ts_add_u32 %11, %15, 0xe000\n\t"
                 "buffer_load_dwordx4 %7, %14, %13, %11 offen sc1\n\t"
                 "buffer_load_dwordx4 %8, %14, %13, %16 offen sc1\n\t"
                 "buffer_load_dwordx4 %9, %14, %13, %16 offen offset:2048 sc1\n\t"
                 "buffer_load_dwordx4 %10, %14, %13, %17 offen sc1\n\t"
                 "1:"
                 : "=&v"(ev[0]), "=&v"(ev[1]), "=&v"(ev[2]), "=&v"(ev[3]), "=&v"(ev[4]), "=&v"(ev[5]), "=&v"(ev[6]), "=&v"(ev[7]),
                   "=&v"(se.x[0]), "=&v"(se.x[1]), "=&v"(se.q), "=&s"(t)
                 : "s"(skip), "s"(rs), "v"(vo), "s"(so), "s"(sx), "s"(sq) : "scc");
}
DI void dma16c_if(int skip, u32x4 rs, unsigned lds, unsigned voff, unsigned soff) {
    asm volatile("s_cmp_lg_u32 %4, 0\n\ts_cbranch_scc1 1f\n\ts_mov_b32 m0, %0\n\ts_nop 0\n\tbuffer_load_dwordx4 %1, %2, %3 offen sc1 lds\n\t1:"
                 :: "s"(lds), "v"(voff), "s"(rs), "s"(soff), "s"(skip) : "scc", "memory");
}
DI void touch_if(unsigned& v, int skip, u32x4 rs, unsigned vo) {
    asm volatile("s_cmp_lg_u32 %1, 0\n\ts_cbranch_scc1 1f\n\tbuffer_load_dword %0, %3, %2, 0 offen\n\t1:" : "+v"(v) : "s"(skip), "s"(rs), "v"(vo) : "scc");
}
DI void ld_flag(unsigned& v, int skip, u32x4 rs, unsigned vf) {
    asm volatile("s_cmp_lg_u32 %1, 0\n\ts_cbranch_scc1 1f\n\tbuffer_load_dword %0, %3, %2, 0 offen sc1\n\t1:" : "=&v"(v) : "s"(skip), "s"(rs), "v"(vf) : "scc");
}
template <int N> DI void chk_flag(unsigned& v, int skip, unsigned tag, u32x4 rs, unsigned vf) {
    unsigned c;
    asm volatile("s_cmp_lg_u32 %2, 0\n\ts_cbranch_scc1 2f\n\ts_waitcnt vmcnt(%6)\n\ts_mov_b32 %1, 0x20000\n\t"
                 "1:\n\tv_cmp_ne_u32 vcc, %3, %0\n\ts_cbranch_vccz 2f\n\ts_sleep 1\n\tbuffer_load_dword %0, %5, %4, 0 offen sc1\n\ts_waitcnt vmcnt(0)\n\t"
                 "s_sub_u32 %1, %1, 1\n\ts_cbranch_scc0 1b\n\t2:"
                 : "+v"(v), "=&s"(c) : "s"(skip), "s"(tag), "s"(rs), "v"(vf), "n"(N) : "scc", "vcc", "memory");
}
DI void st_flag(unsigned tag, int skip, u32x4 rs, unsigned so) {
    unsigned t;
    asm volatile("s_cmp_lg_u32 %1, 0\n\ts_cbranch_scc1 1f\n\tv_mov_b32 %0, %2\n\tbuffer_store_dword %0, off, %3, %4 sc1\n\t1:" : "=&v"(t) : "s"(skip), "s"(tag), "s"(rs), "s"(so) : "scc", "memory");
}
DI void await(unsigned v, unsigned tag, u32x4 rs, unsigned vf) {
#pragma unroll 1
    for (int it = 0; it < (1 << 17); ++it) {
        if (__builtin_amdgcn_ballot_w64(v != tag) == 0) break;
        if (it) __builtin_amdgcn_s_sleep(1);
        asm volatile("buffer_load_dword %0, %1, %2, 0 offen sc1\n\ts_waitcnt vmcnt(0)" : "=v"(v) : "v"(vf), "s"(rs) : "memory");
    }
}
DI void sld3(unsigned& a, u32x4& b4, unsigned& c, const void* pa, const void* pb, const void* pc) {
    asm volatile("s_load_dword %0, %3, 0x0\n\ts_load_dwordx4 %1, %4, 0x0\n\ts_load_dword %2, %5, 0x0\n\ts_waitcnt lgkmcnt(0)"
                 : "=&s"(a), "=&s"(b4), "=&s"(c) : "s"(pa), "s"(pb), "s"(pc) : "memory");
}
template <int AUX> DI void warm(const void* base, unsigned bytes, int first, int step, int end, int pitch, int per, int col) {
    auto rs = __builtin_amdgcn_make_buffer_rsrc((void*)base, 0, bytes, 0x00020000);
    unsigned a = 0;
#pragma unroll 1
    for (int ln = first; ln < end; ln += step) a |= __builtin_amdgcn_raw_buffer_load_b32(rs, (ln / per) * pitch + col + (ln % per) * 128, 0, AUX);
    asm volatile("" :: "v"(a));
}

constexpr unsigned NB = S * D * 2;
#define TP 4096

constexpr unsigned O_FA = 4096, O_FB = O_FA + 32768, O_FP = O_FB + 32768, O_GATE = O_FP + 32768, O_RHO = O_GATE + 8192, O_STAT = O_RHO + 4096;
constexpr unsigned O_X = 524288, O_Q = O_X + NB, O_Y = O_Q + NB, O_H = O_Y + D * TP, O_U = O_H + NB, O_PART = O_U + D * TP, WS_END = O_PART + 8 * NB;
constexpr int SL = 24576, HRING = 5 * SL, AUXO = HRING + 32768;
constexpr int w2slot(int i) { return (i < 3 ? 4 : i < 6 ? 0 : 1) * SL + (i % 3) * 8192; }
constexpr int hslot(int i) { return i % 5 < 3 ? 2 * SL + (i % 5) * 16384 : HRING + (i % 5 - 3) * 16384; }
constexpr int CV_U = 6 * 16640;

__global__ __launch_bounds__(512, 2) void k_all(const u16* __restrict__ x, const u16* __restrict__ kin, const u16* __restrict__ vin, const u16* __restrict__ shk,
                                                const u16* __restrict__ ssk, const u16* __restrict__ ssD, const u16* __restrict__ w1, const u16* __restrict__ w2,
                                                const u16* __restrict__ w3, const u16* __restrict__ w4, const u16* __restrict__ gwt,
                                                char* __restrict__ ws, u16* __restrict__ out) {
    extern __shared__ __attribute__((aligned(16))) char smem[];
    LDSP char* lds = (LDSP char*)smem;
    const int tid = threadIdx.x, l = tid & 63;
    const int w = __builtin_amdgcn_readfirstlane(tid >> 6);
    const int b = blockIdx.x, xc = b & 7, idx = b >> 3;
    const int g = l >> 4, q = (l >> 2) & 3, p = l & 3, r = l & 15;
    const unsigned vl16 = l * 16;
    unsigned sink;
    {
        unsigned long long pc; unsigned len;
        asm volatile("s_getpc_b64 %0\nk_all_code_lo:\n\ts_mov_b32 %1, k_all_code_hi-k_all_code_lo" : "=s"(pc), "=s"(len));
        const u32x4 rc = {(unsigned)pc, (unsigned)(pc >> 32) & 0xffffu, len, 0x00020000u};
        asm volatile("s_cmp_lg_u32 %1, 3\n\ts_cbranch_scc1 1f\n\tbuffer_load_dword %0, %3, %2, 0 offen\n\t1:" : "=&v"(sink) : "s"(w), "s"(rc), "v"((l * 32 + idx) * 128) : "scc");
        SB();
    }
    asm volatile("" : "+s"(x), "+s"(kin), "+s"(vin), "+s"(shk), "+s"(ssk), "+s"(ssD), "+s"(w1), "+s"(w2), "+s"(w3), "+s"(w4), "+s"(gwt), "+s"(ws), "+s"(out));
    const u32x4 rws = mk_rsrc(ws, WS_END);
    const int sw = ((g & 1) << 1) | (q >> 1);
    const int u = w & 3;
    unsigned tag, dD;
    u32x4 k0;
    auto scalars = [&](int c2) {
        sld3(tag, k0, dD, ws, ssk + (size_t)c2 * S, ssD + (c2 & ~1));
        tag = tag * 1664525u + 1013904223u;
        dD = (c2 & 1) ? dD >> 16 : dD & 0xffffu;
    };
    { DECL; STAMP(0);
    if (idx < 18) {
        const int rt = 2 * xc + idx / 9, cb = idx % 9;
        const int kb0 = wrap9(cb + (idx / 9) * 4);
        if (w >= 4) {
            const int lrow = u * 16 + (l >> 3);
            const unsigned vx0 = lrow * 1152 + (((l & 7) ^ g) * 16), vx1 = (lrow + 8) * 1152 + (((l & 7) ^ (4 + g)) * 16);
            const unsigned vw0 = lrow * 1152 + cb * 128 + ((l & 7) ^ ((g & 1) << 1)) * 16, vw1 = (lrow + 8) * 1152 + cb * 128 + ((l & 7) ^ (((g & 1) | 2) << 1)) * 16;
            const u32x4 rx = mk_rsrc(x + (size_t)rt * 128 * D, 128 * D * 2), rw = mk_rsrc(w1, D * D * 2), rw2 = mk_rsrc(w2, D * D * 2), rh = mk_rsrc(ws + O_H, S * D * 2);
            auto issue_x = [&](unsigned dst, int kb, bool first) {
                if (first) {
                    dma16n(rx, dst, vx0, kb * 128); dma16n(rx, dst + 1024, vx1, kb * 128);
                    dma16n(rx, dst + 8192, vx0, kb * 128 + 64 * 1152); dma16n(rx, dst + 8192 + 1024, vx1, kb * 128 + 64 * 1152);
                } else {
                    dma16(rx, dst, vx0, kb * 128); dma16(rx, dst + 1024, vx1, kb * 128);
                    dma16(rx, dst + 8192, vx0, kb * 128 + 64 * 1152); dma16(rx, dst + 8192 + 1024, vx1, kb * 128 + 64 * 1152);
                }
            };
            auto issue = [&](int i) {
                const int kb = wrap9(kb0 + i);
                const unsigned dst = (i % 5) * SL + u * 2048;
                dma16(rw, dst + 16384, vw0, kb * 73728); dma16(rw, dst + 16384 + 1024, vw1, kb * 73728);
                issue_x(dst, kb, i < XNT);
            };
            auto issue_w2 = [&](int i0) {
#pragma unroll
                for (int i = i0; i < i0 + 3; ++i) { const unsigned so = wrap9(kb0 + i) * 73728; dma16(rw2, w2slot(i) + u * 2048, vw0, so); dma16(rw2, w2slot(i) + u * 2048 + 1024, vw1, so); }
            };
            issue(0); bar(); issue(1);
            sfor<0, 7>([&](auto I) { wait_bar<6>(); issue(I.value + 2); });
            wait_bar<6>();
            issue_w2(0);
            wait_bar<6>();
            issue_w2(3);
            bar();
            issue_x(2 * SL + u * 2048, cb, false);
            issue_w2(6);
            scalars(0);
            unsigned vh[9][4], vfh;
#pragma unroll
            for (int i = 0; i < 9; ++i)
#pragma unroll
                for (int e = 0; e < 2; ++e) {
                    const int ck = wrap9(kb0 + i) * 8 + ((l & 7) ^ (4 * e + g));
                    const int sh = ck < 18 ? -1 : ck < 36 ? 1 : ck < 54 ? -2 : 2;
                    vh[i][e] = (unsigned)((rt * 128 + lrow + 8 * e + sh) * 1152 + ck * 16);
                    vh[i][2 + e] = vh[i][e] + 64 * 1152;
                    pin(vh[i][e]); pin(vh[i][2 + e]);
                }
            {
                const int t = min(max(rt + (l < 9 ? 0 : l < 15 ? -1 : 1), 0), 15);
                const int j = l < 9 ? l : l < 15 ? l - 9 + (l >= 12) : l - 13 + (l >= 18);
                const int n = min(l - 21, 35);
                vfh = O_FA + (l < 21 ? ((t & 1) * 9 + j) * 8 + (t >> 1) : (27 + n % 5) * 8 + n / 5) * 128;
                pin(vfh);
            }
            wait_bar<0>();
            if (w == 4) await(~tag, tag, rws, vfh);
            bar();
            auto issue_h = [&](int i) {
#pragma unroll
                for (int e = 0; e < 4; ++e) dma16c(rh, hslot(i) + (e >> 1) * 8192 + u * 2048 + (e & 1) * 1024, vh[i][e], 0);
            };
            dma16c(rws, AUXO + 4608 + min(u, 2) * 1024, vl16, O_RHO + min(u, 2) * 1024);
            issue_h(0); issue_h(1);
            sfor<0, 9>([&](auto I) {
                constexpr int i = I.value;
                wait_bar<(i == 0 ? 4 : i < 7 ? 8 : 4 * (8 - i))>();
                if constexpr (i == 0) issue_h(2);
                if constexpr (i + 3 < 9) issue_h(i + 3);
            });
        } else {
            if (w < 2) dma16(mk_rsrc(gwt, D * 2), AUXO + w * 1024, vl16, w * 1024);
            bar();
            scalars(0);
            if (tid < 20) *(LDSP unsigned*)(lds + AUXO + 2048 + tid * 4) = 0x3f803f80u;
            float mean[2];
            LDSP char* xa[2];
#pragma unroll
            for (int s2 = 0; s2 < 2; ++s2) xa[s2] = lds + (w * 32 + r) * 128 + (((4 * s2 + g) ^ (r >> 1)) * 16);
            {
                LDSP char* xb[2][2]; LDSP char* wb[2][4];
#pragma unroll
                for (int hi = 0; hi < 2; ++hi) {
#pragma unroll
                    for (int s2 = 0; s2 < 2; ++s2) xb[hi][s2] = opaque(xa[s2] + hi * 2 * SL);
#pragma unroll
                    for (int c = 0; c < 4; ++c) wb[hi][c] = opaque(lds + hi * 2 * SL + 16384 + (8 * g + q) * 128 + (((c + w) & 3) ^ sw) * 32 + p * 8);
                }
                LDSP char* ob[9];
#pragma unroll
                for (int i = 0; i < 9; ++i) ob[i] = opaque(lds + AUXO + (r == 1 ? g * 16 + wrap9(kb0 + i) * 128 : 2048));
                u32x4 on = {0x3f803f80u, 0x3f803f80u, 0x3f803f80u, 0x3f803f80u};
                LDSP float* st = (LDSP float*)(lds + AUXO + 4096);
                LDSP char* str_[4]; LDSP char* hwp[4];
                LDSP char* hw = lds + HRING + w * 32 * 144;
#pragma unroll
                for (int c = 0; c < 4; ++c) { str_[c] = opaque((LDSP char*)(st + ((c + w) & 3) * 16 + g * 4)); hwp[c] = opaque(hw + r * 144 + ((c + w) & 3) * 32 + g * 8); }
                LDSP char* hrp = opaque(hw + (l >> 3) * 144 + (l & 7) * 16);
                unsigned vho = (l >> 3) * 1152 + (l & 7) * 16, vdg = ((r >> 2) * 16 + r) * 4, sho = O_H + (rt * 128 + w * 32) * 1152 + cb * 128;
                asm volatile("" : "+v"(vho), "+v"(vdg), "+v"(on));
                asm volatile("" : "+s"(sho));
                f32x4 acc[2][4] = {}, a_sum[2] = {}, a_sq[2] = {}, a_cs = {};
                asm volatile("s_waitcnt vmcnt(0)" : "+v"(acc[0][0]), "+v"(acc[0][1]), "+v"(acc[0][2]), "+v"(acc[0][3]), "+v"(acc[1][0]), "+v"(acc[1][1]), "+v"(acc[1][2]), "+v"(acc[1][3]),
                             "+v"(a_sum[0]), "+v"(a_sum[1]), "+v"(a_sq[0]), "+v"(a_sq[1]), "+v"(a_cs));
                bf16x8 xf[2][2], of[2];
                s16x4 wh[2][4][2];
                float sq[2], gd;
                sfor<0, 19>([&](auto KS) {
                    constexpr int n = KS.value, i = n >> 1, s2 = n & 1, nb = n & 1, a = nb ^ 1;
                    constexpr int sl = i % 5, hi = sl >= 2, off = (sl - 2 * hi) * SL;
                    if constexpr (s2 == 0 && n < 18) {
                        if constexpr (i == 0) lds_bar(); else bar();
                        if constexpr (i == 0) STAMP(1);
                        if constexpr (i == 8) STAMP(2);
                    }
                    auto rd = [&](int e) {
                        if constexpr (n < 18) {
                            if (e < 2) xf[nb][e] = *(LDSP bf16x8*)(xb[hi][s2] + off + e * 2048);
                            else if (e == 2) of[nb] = *(LDSP bf16x8*)(ob[i] + s2 * 64);
                            else if (e < 11) wh[nb][(e - 3) >> 1][(e - 3) & 1] = tr4(wb[hi][(e - 3) >> 1] + off + s2 * 4096 + ((e - 3) & 1) * 512);
                        } else {
                            if (e == 8) {
                                f32x4& c0 = a_cs; f32x4* c1 = a_sq; f32x4* c2 = a_sum;
                                asm volatile("" : "+v"(c0), "+v"(c1[0]), "+v"(c1[1]), "+v"(c2[1]));
                                if (r == 0) *(LDSP f32x4*)str_[0] = a_cs;
                            }
                            if (e == 9) sq[0] = __int_as_float(__builtin_amdgcn_ds_bpermute(vdg, __float_as_int(pick4(a_sq[0], r & 3))));
                            if (e == 10) sq[1] = __int_as_float(__builtin_amdgcn_ds_bpermute(vdg, __float_as_int(pick4(a_sq[1], r & 3))));
                            if (e == 11) gd = __int_as_float(__builtin_amdgcn_ds_bpermute(r * 4, __float_as_int(a_sum[1][1])));
                        }
                        SB();
                    };
#pragma unroll
                    for (int m = 0; m < 13; ++m) {
                        if constexpr (n >= 1) {
                            if (m < 2) mfma1<false>(a_sq[m], xf[a][m], xf[a][m]);
                            else if (m < 4) mfma1<false>(a_sum[m - 2], of[a], xf[a][m - 2]);
                            else if (m == 4) mfma1<false>(a_cs, join(wh[a][0][0], wh[a][0][1]), __builtin_bit_cast(bf16x8, on));
                            else mfma1<false>(acc[(m - 5) & 1][(m - 5) >> 1], join(wh[a][(m - 5) >> 1][0], wh[a][(m - 5) >> 1][1]), xf[a][(m - 5) & 1]);
                            if (n == 18 && m == 8) asm volatile("s_nop 4");
                            SB();
                        }
                        rd(m);
                    }
                });
                lds_bar();
                STAMP(3);
                f32x4 cs[4];
#pragma unroll
                for (int c = 0; c < 4; ++c) cs[c] = *(LDSP f32x4*)str_[c];
                asm volatile("s_nop 4" : "+v"(acc[0][0]), "+v"(acc[0][1]), "+v"(acc[0][2]), "+v"(acc[0][3]), "+v"(acc[1][0]), "+v"(acc[1][1]), "+v"(acc[1][2]), "+v"(acc[1][3]),
                             "+v"(a_sum[0]), "+v"(gd));
                float mr[2], rs[2];
#pragma unroll
                for (int t = 0; t < 2; ++t) {
                    mean[t] = a_sum[t][0] * (1.f / D);
                    rs[t] = GS * __builtin_amdgcn_rsqf(fmaxf(sq[t] * (1.f / D) - mean[t] * mean[t], 0.f) + 1e-5f);
                    mr[t] = -mean[t] * rs[t];
                }
                const f32x2 al = {GALPHA, GALPHA};
                u32x4 o[4];
                SB();
#pragma unroll
                for (int t = 0; t < 2; ++t) {
                    f32x2 uu[8], z[8];
#pragma unroll
                    for (int e = 0; e < 8; ++e) uu[e] = f32x2{cs[e >> 1][(e & 1) * 2], cs[e >> 1][(e & 1) * 2 + 1]} * mr[t];
                    SB();
#pragma unroll
                    for (int e = 0; e < 8; ++e) uu[e] = __builtin_elementwise_fma(f32x2{acc[t][e >> 1][(e & 1) * 2], acc[t][e >> 1][(e & 1) * 2 + 1]}, f32x2{rs[t], rs[t]}, uu[e]);
                    SB();
#pragma unroll
                    for (int e = 0; e < 8; ++e) z[e] = __builtin_elementwise_fma(-uu[e], uu[e], al);
                    SB();
#pragma unroll
                    for (int e = 0; e < 8; ++e) z[e] *= uu[e];
                    SB();
                    if (t == 1) { st16c(o[0], rws, vho, sho); st16c(o[1], rws, vho, sho + 8 * 1152); }
#pragma unroll
                    for (int e = 0; e < 8; ++e) z[e] = f32x2{__builtin_amdgcn_exp2f(z[e][0]), __builtin_amdgcn_exp2f(z[e][1])};
                    SB();
#pragma unroll
                    for (int e = 0; e < 8; ++e) z[e] += 1.f;
                    SB();
#pragma unroll
                    for (int e = 0; e < 8; ++e) z[e] = f32x2{__builtin_amdgcn_rcpf(z[e][0]), __builtin_amdgcn_rcpf(z[e][1])};
                    SB();
#pragma unroll
                    for (int e = 0; e < 8; ++e) z[e] *= uu[e];
                    SB();
#pragma unroll
                    for (int c = 0; c < 4; ++c) *(LDSP u32x2*)(hwp[c] + t * 16 * 144) = u32x2{pk2(z[2 * c][0], z[2 * c][1]), pk2(z[2 * c + 1][0], z[2 * c + 1][1])};
                    o[2 * t] = *(LDSP u32x4*)(hrp + t * 16 * 144); o[2 * t + 1] = *(LDSP u32x4*)(hrp + (t * 16 + 8) * 144);
                    SB();
                }
                st16c(o[2], rws, vho, sho + 16 * 1152); st16c(o[3], rws, vho, sho + 24 * 1152);
                if (cb == 0 && g < 2) st4c(__float_as_uint(rbf(__builtin_amdgcn_rcpf(1.f + __builtin_amdgcn_exp2f(-1.44269504f * rbf(g ? gd : a_sum[0][1]))))), rws, l * 4, O_GATE + (rt * 128 + w * 32) * 4);
                STAMP(4);
            }
            LDSP char* xb[2][2]; LDSP char* wb[2][4]; LDSP char* ob[9];
#pragma unroll
            for (int hi = 0; hi < 2; ++hi) {
#pragma unroll
                for (int s2 = 0; s2 < 2; ++s2) xb[hi][s2] = opaque(xa[s2] + (hi ? HRING : 2 * SL));
#pragma unroll
                for (int c = 0; c < 4; ++c) wb[hi][c] = opaque(lds + hi * 4 * SL + (8 * g + q) * 128 + (c ^ sw) * 32 + p * 8);
            }
#pragma unroll
            for (int i = 0; i < 9; ++i) ob[i] = opaque(lds + AUXO + 4608 + (r & 1) * 1152 + g * 16 + wrap9(kb0 + i) * 128);
            unsigned sxo = O_X + ((rt * 2 + (w >> 1)) * 9 + cb) * 8192 + (w & 1) * 4096, sso = O_STAT + ((rt * 4 + w) * 9 + cb) * 256;
            asm volatile("" : "+s"(sxo), "+s"(sso));
            wait_bar<0>();
            if (tid == 0) st4c(tag, rws, 0, O_FA + b * 128);
            STAMP(5);
            f32x4 acc[2][4], a_m[2] = {};
#pragma unroll
            for (int c = 0; c < 4; ++c) {
                u32x2 xv[2];
#pragma unroll
                for (int t = 0; t < 2; ++t) {
                    xv[t] = *(LDSP u32x2*)(lds + 2 * SL + (w * 32 + t * 16 + r) * 128 + (((c * 2 + (g >> 1)) ^ (r >> 1)) * 16) + (g & 1) * 8);
                    acc[t][c] = (f32x4{bflo(xv[t][0]), bfhi(xv[t][0]), bflo(xv[t][1]), bfhi(xv[t][1])} - mean[t]) * GS;
                }
                st16c(u32x4{xv[0][0], xv[0][1], xv[1][0], xv[1][1]}, rws, vl16, O_Q + ((rt * 4 + w) * 36 + cb * 4 + c) * 1024);
            }
            asm volatile("" : "+v"(acc[0][0]), "+v"(acc[0][1]), "+v"(acc[0][2]), "+v"(acc[0][3]), "+v"(acc[1][0]), "+v"(acc[1][1]), "+v"(acc[1][2]), "+v"(acc[1][3]), "+v"(a_m[0]), "+v"(a_m[1]));
            bf16x8 wr[18][WREG];
#pragma unroll
            for (int n = 0; n < 18; ++n)
#pragma unroll
                for (int c = 0; c < WREG; ++c) wr[n][c] = ld_tr(wb[n < 6][c] + w2slot(n >> 1) - (n < 6 ? 4 * SL : 0) + (n & 1) * 4096, 512);
            lds_bar();
            STAMP(6);
            bf16x8 xf[2][2], of[2];
            s16x4 wh[2][4][2];
            sfor<0, 19>([&](auto KS) {
                constexpr int n = KS.value, i = n >> 1, s2 = n & 1, nb = n & 1, a = nb ^ 1;
                constexpr int xhi = i % 5 >= 3, xoff = hslot(i) - (xhi ? HRING : 2 * SL), whi = i < 3, woff = w2slot(i) - (whi ? 4 * SL : 0);
                if constexpr (s2 == 0 && n < 18) {
                    bar();
                    if constexpr (i == 0) STAMP(7);
                    if constexpr (i == 8) STAMP(8);
                }
                auto rd = [&](int e) {
                    if constexpr (n < 18) {
                        if (e < 2) xf[nb][e] = *(LDSP bf16x8*)(xb[xhi][s2] + xoff + e * 2048);
                        else if (e == 2) of[nb] = *(LDSP bf16x8*)(ob[i] + s2 * 64);
                        else if (e < 11 - 2 * WREG) { const int f = e - 3 + 2 * WREG; wh[nb][f >> 1][f & 1] = tr4(wb[whi][f >> 1] + woff + s2 * 4096 + (f & 1) * 512); }
                    }
                    SB();
                };
#pragma unroll
                for (int m = 0; m < 10; ++m) {
                    if constexpr (n >= 1) {
                        if (m < 2 * WREG) mfma1<false>(acc[m & 1][m >> 1], wr[n - 1][(m >> 1) % WREG], xf[a][m & 1]);
                        else if (m < 2 * WREG + 2) mfma1<false>(a_m[m & 1], of[a], xf[a][m & 1]);
                        else mfma1<false>(acc[m & 1][(m - 2) >> 1], join(wh[a][(m - 2) >> 1][0], wh[a][(m - 2) >> 1][1]), xf[a][m & 1]);
                        SB();
                    }
                    rd(m);
                }
            });
            asm volatile("s_nop 7" : "+v"(acc[0][0]), "+v"(acc[0][1]), "+v"(acc[0][2]), "+v"(acc[0][3]), "+v"(acc[1][0]), "+v"(acc[1][1]), "+v"(acc[1][2]), "+v"(acc[1][3]), "+v"(a_m[0]), "+v"(a_m[1]));
            STAMP(9);
            f32x4 s2[2] = {};
            float mh[2];
#pragma unroll
            for (int t = 0; t < 2; ++t) mh[t] = a_m[t][0] + a_m[t][1];
#pragma unroll
            for (int m = 0; m < 2; ++m)
#pragma unroll
                for (int t = 0; t < 2; ++t) {
                    const f32x4 a0 = acc[t][2 * m] - mh[t], a1 = acc[t][2 * m + 1] - mh[t];
                    const u32x4 xo = {pk2(a0[0], a0[1]), pk2(a0[2], a0[3]), pk2(a1[0], a1[1]), pk2(a1[2], a1[3])};
                    st16c(xo, rws, vl16, sxo + t * 2048 + m * 1024);
                    s2[t] = mfma(__builtin_bit_cast(bf16x8, xo), __builtin_bit_cast(bf16x8, xo), s2[t]);
                }
            const u32x4 so = {__float_as_uint(pick4(s2[0], r & 3)), __float_as_uint(pick4(s2[1], r & 3)), __float_as_uint(mean[0] * GS + mh[0]), __float_as_uint(mean[1] * GS + mh[1])};
            if (g == (r >> 2)) st16c(so, rws, r * 16, sso);
            STAMP(10);
        }
    } else {
        const int j = idx - 18, nx = j < 12 ? 1 : 2;
        const int xs = nx == 2 ? u >> 1 : 0;
        const int chm = xc * 72 + j * 4 + u, chx0 = xc * 72 + 56 + (j < 12 ? j : 2 * j - 12);
        const int ch = w < 4 ? chm : chx0 + xs, slot = w < 4 ? u : 4 + xs;
        const u32x4 rk = mk_rsrc(ssk + (size_t)chm * S, S * 2), rk0 = mk_rsrc(ssk + (size_t)chx0 * S, S * 2), rk1 = mk_rsrc(ssk + (size_t)(chx0 + nx - 1) * S, S * 2);
        auto conv_k = [&](int c) {
            auto d = [&](u32x4 rs, unsigned dst, unsigned so) { dma16(rs, dst + c * 4160 + 64, vl16 + 4 + 4 * c, so); };
#pragma unroll
            for (int i = 0; i < 4; ++i) d(rk, u * 16640 + i * 1024, i * 1024);
            d(rk0, 4 * 16640 + u * 1024, u * 1024);
            if (nx == 2) d(rk1, 5 * 16640 + u * 1024, u * 1024);
        };
        if (idx < 27) {
            const int tb = xc, c0 = j * 64;
            const int rg = l >> 3, c = l & 7;
            constexpr int TO = 83968;
            u32x4 sk[4];
            LDSP char* kb = lds + w * 9 * 1152 + c * 16;
            {
                const u32x4 rsk = mk_rsrc(shk, D * 8), rki = mk_rsrc(kin, S * D * 2), rvi = mk_rsrc(vin, S * D * 2);
#pragma unroll
                for (int i = 0; i < 4; ++i) ld16(sk[i], rsk, (c0 + c * 8) * 8, i * 16);
                const unsigned vo = (tb * 256 + w * 32 + (l >> 3)) * 1152 + c0 * 2 + (l & 7) * 16;
                dma16n(rki, w * 9 * 1152, vo - 9216, 0);
#pragma unroll
                for (int i = 0; i < 4; ++i) { dma16n(rki, (w * 9 + 1 + i) * 1152, vo, i * 9216); dma16n(rvi, (w * 9 + 5 + i) * 1152, vo, i * 9216); }
            }
            scalars(ch);
            asm volatile("s_waitcnt vmcnt(9)" : "+v"(sk[0]), "+v"(sk[1]), "+v"(sk[2]), "+v"(sk[3]) :: "memory");
            f32x2 sf[4][4];
#pragma unroll
            for (int e = 0; e < 4; ++e) {
                sf[e][0] = f32x2{bflo(sk[e][0]), bflo(sk[e][2])}; sf[e][1] = f32x2{bfhi(sk[e][0]), bfhi(sk[e][2])};
                sf[e][2] = f32x2{bflo(sk[e][1]), bflo(sk[e][3])}; sf[e][3] = f32x2{bfhi(sk[e][1]), bfhi(sk[e][3])};
                asm volatile("" : "+v"(sf[e][0]), "+v"(sf[e][1]), "+v"(sf[e][2]), "+v"(sf[e][3]));
            }
            wait_vm<0>();
            STAMP(1);
            u32x4 kr[7], vr[4];
#pragma unroll
            for (int i = 0; i < 7; ++i) { const int jj = rg + 1 + ((i + 1) >> 2); kr[i] = *(LDSP u32x4*)(kb + (jj >> 1) * 1152 + ((jj & 1) * 4 + ((i + 1) & 3)) * 128); }
#pragma unroll
            for (int i = 0; i < 4; ++i) vr[i] = *(LDSP u32x4*)(kb + (5 + (rg >> 1)) * 1152 + ((rg & 1) * 4 + i) * 128);
            LDSP char* tw = lds + TO + c * (8 * 512 + 16) + (63 - w * 8 - rg) * 8;
#pragma unroll
            for (int e = 0; e < 4; ++e) {
                f32x2 kk[7], uu[4];
#pragma unroll
                for (int i = 0; i < 7; ++i) kk[i] = f32x2{bflo(kr[i][e]), bfhi(kr[i][e])};
#pragma unroll
                for (int i = 0; i < 4; ++i) uu[i] = kk[i] * sf[e][3];
#pragma unroll
                for (int jj = 1; jj < 4; ++jj)
#pragma unroll
                    for (int i = 0; i < 4; ++i) uu[i] = __builtin_elementwise_fma(kk[i + jj], sf[e][3 - jj], uu[i]);
#pragma unroll
                for (int i = 0; i < 4; ++i) uu[i] *= f32x2{bflo(vr[i][e]), bfhi(vr[i][e])};
#pragma unroll
                for (int z = 0; z < 2; ++z) *(LDSP u32x2*)(tw + (2 * e + z) * 512) = u32x2{pk2(uu[3][z], uu[2][z]), pk2(uu[1][z], uu[0][z])};
            }
            STAMP(2);
            lds_bar();
            STAMP(3);
            u32x4 uo[4];
#pragma unroll
            for (int i = 0; i < 4; ++i) { const int cl = i * 16 + (tid >> 5); uo[i] = *(LDSP u32x4*)(lds + TO + cl * 512 + (cl >> 3) * 16 + (tid & 31) * 16); }
#pragma unroll
            for (int i = 0; i < 4; ++i) st16c(uo[i], rws, (tid >> 5) * TP + (tid & 31) * 16, O_U + (c0 + i * 16) * TP + (1792 - tb * 256) * 2);
            STAMP(4);
            wait_bar<0>();
            if (tid == 0) st4c(tag, rws, 0, O_FA + b * 128);
        } else {
            const int n = xc * 5 + idx - 27, row = n * 16 + w * 2 + (l >> 5);
            u32x4 wv[3];
            const u32x4 rw2 = mk_rsrc(w2, D * D * 2);
#pragma unroll
            for (int i = 0; i < 3; ++i) ld16(wv[i], rw2, (i == 2 && (l & 31) >= 8) ? 0x40000000u : row * 1152 + (l & 31) * 16, i * 512);
            scalars(ch);
            asm volatile("s_waitcnt vmcnt(0)" : "+v"(wv[0]), "+v"(wv[1]), "+v"(wv[2]));
            float sm = 0.f;
#pragma unroll
            for (int i = 0; i < 12; ++i) sm += bflo(wv[i >> 2][i & 3]) + bfhi(wv[i >> 2][i & 3]);
#pragma unroll
            for (int m = 1; m < 32; m <<= 1) sm += __shfl_xor(sm, m);
            sm *= 1.f / D;
            const float hi = rbf(sm);
            if (n < 36 && (l & 31) < 2) st2c(pk2((l & 1) ? sm - hi : hi, 0.f), rws, row * 2 + (l & 1) * 1152, O_RHO);
            wait_bar<0>();
            if (tid == 0) st4c(tag, rws, 0, O_FA + b * 128);
            warm<0>(ssk + (size_t)xc * 72 * S, 72 * S * 2, (idx - 27) * 512 + tid, 2560, 72 * 32, 4096, 32, 0);
        }
        STAMP(5);
        if (w >= 4) { conv_k(0); conv_k(1); conv_k(2); conv_k(3); }
        if (l < 16) {
            const int c = l >> 2;
            u32x4 o = {0u, 0u, 0u, 0u};
            if ((l & 3) == 3) {
                o[3] = c == 0 ? k0[0] : c == 1 ? k0[1] : c == 2 ? k0[2] : k0[3];
                o[2] = c == 0 ? 0u : c == 1 ? k0[0] : c == 2 ? k0[1] : k0[2];
                o[1] = c <= 1 ? 0u : c == 2 ? k0[0] : k0[1];
                o[0] = c <= 2 ? 0u : k0[0];
            }
            *(LDSP u32x4*)(lds + slot * 16640 + c * 4160 + (l & 3) * 16) = o;
        }
        if (l < 56) *(LDSP u32x4*)(lds + CV_U + slot * 9152 + (l / 28) * 4576 + 4096 + (l % 28) * 16) = u32x4{0u, 0u, 0u, 0u};
        const int trips = w < 4 ? 4 : nx, t0 = w < 4 ? 0 : nx == 2 ? (u & 1) * 4 : u * 2;
        LDSP char* ab = opaque(lds + slot * 16640 + (r & 3) * 4160 + ((r >> 2) + g) * 16 + t0 * 64);
        LDSP char* bb = opaque(lds + CV_U + slot * 9152 + (r & 1) * 4576 + (28 - 4 * (r >> 1) + g) * 16 + t0 * 64);
        u32x4 dv;
#pragma unroll
        for (int e = 0; e < 4; ++e) dv[e] = (30 - 2 * r - 8 * g == 2 * e && t0 == 0) ? dD : 0u;
        asm volatile("" : "+v"(dv));
        const bf16x8 da = __builtin_bit_cast(bf16x8, dv);
        auto put_y = [&](const f32x4& a0, const f32x4& a1, int cy, int cp) {
            st8c(u32x2{pk2(a0[0], a0[1]), pk2(a0[2], a0[3])}, rws, l * 8, O_Y + cy * TP + cp * 1024);
            st8c(u32x2{pk2(a1[0], a1[1]), pk2(a1[2], a1[3])}, rws, l * 8, O_Y + cy * TP + cp * 1024 + 512);
        };
        unsigned vt[3], snk = 0;
#pragma unroll
        for (int i = 0; i < 3; ++i) { const int ln = (i * 14 + j) * 256 + tid; vt[i] = (ln / 18) * 9216 + (xc & 3) * 2304 + (ln % 18) * 128; pin(vt[i]); }
        const u32x4 rw3 = mk_rsrc(w3, D * F * 2);
        if (w < 4) {
            await(~tag, tag, rws, O_FA + ((18 + ((l < 8 ? chm : chx0 + xs) >> 6)) * 8 + (l & 7)) * 128);
            STAMP(6);
            u32x4 ur[6]; unsigned nw[6];
            const int e0 = nx == 2 ? (u & 1) * 2048 : u * 1024, e1 = nx == 2 ? e0 + 1024 : e0;
            const u32x4 rux = mk_rsrc(ws + O_U + (chx0 + xs) * TP, 4096);
#pragma unroll
            for (int i = 0; i < 4; ++i) ld16c(ur[i], rws, vl16, O_U + chm * TP + i * 1024);
            ld16c(ur[4], rux, vl16, e0); ld16c(ur[5], rux, vl16, e1);
            ld4c(nw[5], rux, 1024, e1);
            asm volatile("s_waitcnt vmcnt(0)" : "+v"(ur[0]), "+v"(ur[1]), "+v"(ur[2]), "+v"(ur[3]), "+v"(ur[4]), "+v"(ur[5]), "+v"(nw[5]));
            STAMP(8);
#pragma unroll
            for (int i = 0; i < 3; ++i) nw[i] = __builtin_amdgcn_readfirstlane(ur[i + 1][0]);
            nw[3] = 0u; nw[4] = nx == 2 ? __builtin_amdgcn_readfirstlane(ur[5][0]) : nw[5];
#pragma unroll
            for (int i = 0; i < 6; ++i) {
                LDSP char* uw = lds + CV_U + l * 16 + (i < 4 ? u * 9152 + i * 1024 : (4 + xs) * 9152 + (i == 4 ? e0 : e1));
                const unsigned nx1 = __builtin_amdgcn_update_dpp(nw[i], ur[i][0], 0x130, 0xf, 0xf, false);
                if (i == 5 && nx == 1) break;
                *(LDSP u32x4*)(uw + 4576) = ur[i];
                *(LDSP u32x4*)uw = u32x4{__builtin_amdgcn_alignbit(ur[i][1], ur[i][0], 16), __builtin_amdgcn_alignbit(ur[i][2], ur[i][1], 16),
                                         __builtin_amdgcn_alignbit(ur[i][3], ur[i][2], 16), __builtin_amdgcn_alignbit(nx1, ur[i][3], 16)};
            }
        }
        LDSP unsigned* cnt = (LDSP unsigned*)(lds + 163836);
        if (tid == 256) *cnt = 0u;
        if (w >= 4) __builtin_amdgcn_s_setprio(1);
        wait_bar_l<0>();
        STAMP(7);
        f32x4 acc[8];
        {
            bf16x8 A[2][8], B[2][8];
            auto ldf = [&](int t, int e) {
                if (e & 1) A[t & 1][e >> 1] = *(LDSP bf16x8*)(ab + (t + 8 * (e >> 1)) * 64);
                else B[t & 1][e >> 1] = *(LDSP bf16x8*)(bb + (7 - (e >> 1)) * 512 + t * 64);
            };
#pragma unroll
            for (int e = 0; e < 16; ++e) ldf(0, e);
#pragma unroll
            for (int n = 0; n < 8; ++n) mfma1<true>(acc[n], B[0][n], da);
            SB();
#pragma unroll 1
            for (int it = 0; it < trips; ++it) {
                if (it >= TIT) {
                    if (w < 4 && it < TIT + 3) asm volatile("buffer_load_dword %0, %1, %2, 0 offen" TNT : "+v"(snk) : "v"(vt[0]), "s"(rw3));
                    vt[0] = vt[1]; vt[1] = vt[2];
                }
#pragma unroll
                for (int t = 0; t < 2; ++t) {
                    int cnt = 0;
#pragma unroll
                    for (int m = 0; m < 8; ++m)
#pragma unroll
                        for (int n = 0; n + m < 8; ++n) {
                            mfma1<false>(acc[m + n], B[t][n], A[t][m]);
                            if ((cnt & 1) == 0 && cnt < 32) ldf(t + 1, cnt >> 1);
                            ++cnt;
                            SB();
                        }
                }
                ab += 128; bb += 128;
            }
        }
        asm volatile("s_waitcnt lgkmcnt(0)\n\ts_nop 7" : "+v"(acc[0]), "+v"(acc[1]), "+v"(acc[2]), "+v"(acc[3]), "+v"(acc[4]), "+v"(acc[5]), "+v"(acc[6]), "+v"(acc[7]) :: "memory");
        STAMP(9);
        if (w < 4) {
#pragma unroll
            for (int i = 0; i < 4; ++i) put_y(acc[2 * i], acc[2 * i + 1], chm, i);
            asm volatile("s_waitcnt vmcnt(8)" :: "v"(snk));
        } else {
            auto meet = [&](unsigned n) {
                asm volatile("s_waitcnt lgkmcnt(0)" ::: "memory");
                if (l == 0) __hip_atomic_fetch_add(cnt, 1u, __ATOMIC_RELAXED, __HIP_MEMORY_SCOPE_WORKGROUP);
#pragma unroll 1
                for (int it = 0; it < (1 << 20) && __hip_atomic_load(cnt, __ATOMIC_RELAXED, __HIP_MEMORY_SCOPE_WORKGROUP) < n; ++it) __builtin_amdgcn_s_sleep(1);
                asm volatile("" ::: "memory");
            };
            LDSP char* pb = lds + 4 * 16640 + l * 16;
            __builtin_amdgcn_s_setprio(0);
            meet(4);
#pragma unroll
            for (int c = 0; c < 8; ++c) *(LDSP f32x4*)(pb + (u * 8 + c) * 1024) = acc[c];
            meet(8);
            f32x4 pp[2][4];
#pragma unroll
            for (int i = 0; i < 8; ++i) pp[i & 1][i >> 1] = *(LDSP f32x4*)(pb + ((i >> 1) * 8 + u * 2 + (i & 1)) * 1024);
            const f32x4 a0 = pp[0][0] + pp[0][1], a1 = pp[1][0] + pp[1][1], b0 = pp[0][2] + pp[0][3], b1 = pp[1][2] + pp[1][3];
            if (nx == 1) put_y(a0 + b0, a1 + b1, chx0, u);
            else { put_y(a0, a1, chx0, u); put_y(b0, b1, chx0 + 1, u); }
        }
        STAMP(10);
    }
    DUMP(0, 12); }
    const int fc = 2 * (xc & 3) + (idx & 1), rt = (xc >> 2) * 16 + (idx >> 1);
    const int wi = w >> 2, wj = w & 3;
    const int rho = rt % 9;
    DECL;
    const u32x4 r3 = mk_rsrc((const char*)w3 + fc * 1152 + (size_t)w * 8 * 9216, D * F * 2 - (fc * 1152 + w * 8 * 9216));
    const u32x4 r4 = mk_rsrc((const char*)w4 + (size_t)(fc * 576 + w * 8) * 1152, D * F * 2 - (fc * 576 + w * 8) * 1152);
    const unsigned vsw = ((l & 7) ^ (g << 1)) * 16;
    const unsigned vo3 = (l >> 3) * 9216 + vsw, vo4 = (l >> 3) * 1152 + vsw;
    const unsigned ldsw = w * 1024;
    auto sigma = [&](int pos) { return pos == 0 ? 8 : ((fc + pos) & 7); };
    auto issue = [&](auto N) {
        constexpr int n = N.value, st = n / 9, kk = n % 9;
        if constexpr (st < 9) dma16(r3, ldsw + (st & 1) * HALF + kk * 8192, vo3, kk * 64 * 9216 + wrap9(st + rho) * 128);
        else dma16(r4, ldsw + (st & 1) * HALF + kk * 8192, vo4, wrap9(kk + rho) * 73728 + wrap9(sigma(st - 9) + rho) * 128);
    };
    unsigned fl, vxf = O_FB + ((((rt >> 1) & 1) * 9 + min(l, 8)) * 8 + (rt >> 2)) * 128;
    pin(vxf);
    lds_bar();
    if (idx >= 18) ld_flag(fl, w, rws, vxf); else fl = ~tag;
    sfor<0, 3>(issue);
    if (idx < 18) {
        wait_bar<3>();
        st_flag(tag, w, rws, O_FB + b * 128);
    }
    STAMP(0);
    sfor<3, 9>(issue);
    const int cs_own = wrap9(fc + rho), cs_sh = wrap9(8 + rho);
    auto yflag = [&](int c) { const int cl = c % 72; return O_FB + ((18 + (cl < 56 ? cl >> 2 : cl < 68 ? cl - 56 : (cl - 44) >> 1)) * 8 + c / 72) * 128; };
    unsigned vyf = yflag(w == 0 ? cs_own * 64 + l : cs_sh * 64 + (fc & 3) * 16 + r);
    unsigned vpf = O_FP + (((rt & 15) * 2 + (l & 1)) * 8 + (rt >> 4) * 4 + ((l >> 1) & 3)) * 128;
    if (w >= 4) vpf = O_FB + (tid - 256) * 128;
    const int pskip = w == 0 ? 0 : w >= 4 ? b : 1;
    unsigned vy = (l >> 3) * TP + (l & 7) * 16, vg = r * 4;
    pin(vyf); pin(vpf); pin(vy); pin(vg);
    const u32x4 rt4 = mk_rsrc((const char*)w4 + (size_t)(fc * 576 + rt * 18) * 1152, 18 * 1152);
    unsigned vt4 = (l * 8 + w) * 128;
    pin(vt4);
    LDSP char* gr = lds + wi * 4096 + l * 16;
    LDSP char* gw = opaque(lds + wi * 4096 + (wj >> 1) * 1024 + l * 16 + (wj & 1) * 8);
    LDSP char* tb = lds + (4 * g + q) * 128 + (wj ^ sw) * 32 + p * 8;
    LDSP char* tbs[3] = {opaque(tb), opaque(tb + 65536), opaque(tb + 131072)};
    LDSP char* grs[3] = {opaque(gr), opaque(gr + 65536), opaque(gr + 131072)};
    chk_flag<9>(fl, w, tag, rws, vxf);
    bar();
    STAMP(1);
    u32x4 sr[3];
    auto issue_x = [&](int i) { dma16c(rws, HALF + i * 8192 + ldsw, vl16, O_X + (rt * 9 + i) * 8192 + ldsw); };
#pragma unroll
    for (int i = 0; i < XPRE; ++i) { if (i == 4) bar(); issue_x(i); }
    STAMP(2);
    bf16x8 X[36];
    auto ldw = [&](auto KS) -> bf16x8 {
        constexpr int st = KS.value / 18, ks = KS.value % 18, off = (st & 1) * HALF + ks * 4096;
        return ld_tr(tbs[off >> 16] + (off & 65535), 2048);
    };
    auto ldx = [&](auto KS) {
        constexpr int gk = KS.value, ks = gk % 18;
        if constexpr (gk < 18 || (gk >= 162 && gk < 180)) {
            constexpr int off = (gk < 18 ? HALF : 0) + (ks >> 1) * 8192 + (ks & 1) * 1024;
            LDSP char* a = grs[off >> 16] + (off & 65535);
            X[2 * ks] = *(LDSP bf16x8*)a; X[2 * ks + 1] = *(LDSP bf16x8*)(a + 2048);
        }
    };
    bf16x8 Wf[3];
    unsigned Gown[9][4];
    f32x4 acc[2][2];
    GeluT T;
    auto putg = [&](int s, int t) { *(LDSP u32x2*)(gw + s * 8192 + t * 2048) = u32x2{Gown[s][2 * t], Gown[s][2 * t + 1]}; };
    const unsigned pt = O_PART + rt * 9 * 8 * 8 * 1024 + w * 1024;
    auto mine = [&](int pos) { return pt + (unsigned)((sigma(pos) * 8 + fc) * 8192); };
    auto theirs = [&](int pos) { return pt + (unsigned)((fc * 8 + ((fc - pos) & 7)) * 8192); };
    auto sx_of = [&](int cs) { return O_X + (unsigned)((rt * 9 + cs) * 8192 + wi * 4096 + (wj >> 1) * 1024); };
    auto sq_of = [&](int cs) { return O_Q + (unsigned)(((rt * 2 + wi) * 36 + cs * 4 + wj) * 1024); };
    constexpr int YO = 2 * HALF, YS = YO + 9728;
    auto y_at = [&](int off, int t, int e) { return (unsigned)*(LDSP u16*)(lds + off + (4 * g + e) * 128 + t * 64 + (r >> 1) * 8 + wi * 4 + (r & 1) * 2); };
    Side sd, se;
    u32x4 pv[8], ev[8], sel[3];
    unsigned gt[2], yv[2][2];
    float base[8], rs[2], mu[2], ngs[2];

    sfor<0, 18>([&](auto ST) {
        constexpr int st = ST.value;
        sfor<0, 9>([&](auto KK) {
            constexpr int kk = KK.value, n = st * 9 + kk, gk = 2 * n;
            if constexpr (is_bar(n)) {
                static_assert(SCH.vm[n] >= 0 && SCH.vm[n] < 64, "schedule");
                if constexpr (n == N_YCHK || n == N_CHK) {
                    chk_flag<(n == N_CHK ? SCH.pollwait : SCH.ywait)>(fl, n == N_CHK ? pskip : w >> 1, tag, rws, n == N_CHK ? vpf : vyf);
                }
                if constexpr (n == 81 || n == 84) wait_bar_l<SCH.vm[n]>(); else wait_bar<SCH.vm[n]>();
                if constexpr (n == 3) { st_flag(tag, idx < 18 ? 1 : w, rws, O_FB + b * 128); pin(sink); }
                if constexpr (n == N_PUB) st_flag(tag, w, rws, O_FP + b * 128);
                if constexpr (n == N_CHK) st_flag(tag, b | w, rws, 0);
                if constexpr (n == 0) { ldx(IC(0)); Wf[0] = ldw(IC(0)); ldx(IC(1)); Wf[1] = ldw(IC(1)); }
                if constexpr (n == 81) { ldx(IC(162)); ldx(IC(163)); }
            }
            if constexpr (n == N_YPOLL) ld_flag(fl, w >> 1, rws, vyf);
            if constexpr (n == N_POLL) ld_flag(fl, pskip, rws, vpf);
            if constexpr (kk == 0) STAMP(3 + st);
            SB();
            f32x4* A = acc[st & 1];
            f32x4* P = acc[(st & 1) ^ 1];
            auto gput = [&](int c) {
                if constexpr (n >= 78 && n < 81) { putg(2 * (n - 78) + (c >> 1), c & 1); SB(); }
                if constexpr (n == 81 || n == 82) { if (c & 1) { putg(6 + n - 81, c >> 1); SB(); } }
                if constexpr (n == 83) { if (c == 1) { putg(8, 0); SB(); } }
            };
            const bf16x8 Wa = Wf[gk % 3], Wb = Wf[(gk + 1) % 3];
            mfma1<kk == 0>(A[0], Wa, X[4 * kk]);
            SB();
            if constexpr (n < 161) { Wf[(gk + 2) % 3] = ldw(IC(gk + 2)); if constexpr (n != 80) ldx(IC(gk + 2)); SB(); }
            gput(0);
            mfma1<kk == 0>(A[1], Wa, X[4 * kk + 1]);
            SB();
            if constexpr (n < 3 && XPRE + 2 * n < 9) { issue_x(XPRE + 2 * n); SB(); }
            if constexpr (SCH.d1[n] >= 0) { issue(IC(SCH.d1[n] >= 0 ? SCH.d1[n] : 0)); SB(); }
            gput(1);
            mfma1<false>(A[0], Wb, X[4 * kk + 2]);
            SB();
            if constexpr (n < 161) { Wf[gk % 3] = ldw(IC(gk + 3)); if constexpr (n != 80) ldx(IC(gk + 3)); SB(); }
            gput(2);
            mfma1<false>(A[1], Wb, X[4 * kk + 3]);
            SB();
            if constexpr (n < 3 && XPRE + 2 * n + 1 < 9) { issue_x(XPRE + 2 * n + 1); SB(); }
            if constexpr (n == 2) {
#pragma unroll
                for (int i = 0; i < 3; ++i) ld16c(sr[i], rws, (i == 2 && g) ? 0x40000000u : vl16, O_STAT + (rt * 2 + wi) * 2304 + i * 1024);
            }
            if constexpr (SCH.d2[n] >= 0) { issue(IC(SCH.d2[n] >= 0 ? SCH.d2[n] : 0)); SB(); }
            gput(3);
            if constexpr (st >= 1 && st <= 8) gelu_blk<kk>(T, P, rs, Gown[st - 1]);
            if constexpr (n == 81) Gown[8][0] = gelu2(P[0][0], P[0][1], rs[0]);
            if constexpr (n == 82) { Gown[8][1] = gelu2(P[0][2], P[0][3], rs[0]); Gown[8][2] = gelu2(P[1][0], P[1][1], rs[1]); }
            if constexpr (n == 83) { Gown[8][3] = gelu2(P[1][2], P[1][3], rs[1]); putg(8, 1); }
            if constexpr (n == 8) {
                u32x4* s3 = sr;
                asm volatile("" : "+v"(s3[0]), "+v"(s3[1]), "+v"(s3[2]));
#pragma unroll
                for (int t = 0; t < 2; ++t) {
                    const float s2 = sum_g(__uint_as_float(sr[0][t]) + __uint_as_float(sr[1][t]) + __uint_as_float(sr[2][t]));
                    rs[t] = GS * __builtin_amdgcn_rsqf(s2 * (1.f / D) + 1e-5f * GS * GS);
                    mu[t] = __uint_as_float(sr[0][2 + t]);
                }
                float* r2 = rs;
                asm volatile("" : "+v"(r2[0]), "+v"(r2[1]));
            }
            if constexpr (n_store(n)) {
                u32x4 o = {pk2(P[0][0], P[0][1]), pk2(P[0][2], P[0][3]), pk2(P[1][0], P[1][1]), pk2(P[1][2], P[1][3])};
                if constexpr (st >= 13) {
#pragma unroll
                    for (int e = 0; e < 4; ++e) o[e] = min(o[e], 0xfffffffeu);
                }
                st16c(o, rws, vl16, mine(st - 10));
            }
            if constexpr (fill_at(n) >= 0) st16c(u32x4{~0u, ~0u, ~0u, ~0u}, rws, vl16, mine(fill_at(n)));
            if constexpr (n >= T0 && n < T0 + 16 * TS && (n - T0) % TS == 0) touch_if(sink, (rt & 15) - (n - T0) / TS, rt4, vt4);
            if constexpr (n == 90) pin(sink);
            if constexpr (n == N_SIDE) {
                dma16c_if(w >> 1, rws, YS + ldsw, vy, O_Y + (cs_sh * 64 + (fc & 3) * 16 + w * 8) * TP + rt * 128);
                dma16c(rws, YO + ldsw, vy, O_Y + (cs_own * 64 + w * 8) * TP + rt * 128);
                ld16c(sd.q, rws, vl16, sq_of(cs_own));
            }
            if constexpr (n == N_SIDE + 1) { ld16c(sd.x[0], rws, vl16, sx_of(cs_own)); ld16c(sd.x[1], rws, vl16, sx_of(cs_own) + 2048); }
            if constexpr (n == N_SIDE + 2) { ld4c(gt[0], rws, vg, O_GATE + (rt * 64 + wi * 32) * 4); ld4c(gt[1], rws, vg, O_GATE + (rt * 64 + wi * 32 + 16) * 4); }
            if constexpr (n >= 100 && n < 104) { yv[n & 1][0] = y_at(YO + wj * 2048, (n - 100) >> 1, (n & 1) * 2); yv[n & 1][1] = y_at(YO + wj * 2048, (n - 100) >> 1, (n & 1) * 2 + 1); }
            if constexpr (n == 100) {
                pin(gt[0]); pin(gt[1]);
#pragma unroll
                for (int t = 0; t < 2; ++t) { ngs[t] = rbf(1.f - __uint_as_float(gt[t])) * GINV; mu[t] *= ngs[t]; }
            }
            if constexpr (n >= 101 && n < 105) {
                constexpr int e = n - 101;
                mix_base(base[2 * e], base[2 * e + 1], sd.q[e], yv[e & 1][0], yv[e & 1][1], __uint_as_float(gt[e >> 1]), mu[e >> 1]);
            }
            sfor<1, 8>([&](auto I) { if constexpr (get_n(I.value) == n) ld16c(pv[I.value], rws, vl16, theirs(I.value)); });
            if constexpr (n == NSH) { ld_shared(ev, se, w - fc, rws, vl16, pt + 64 * 8192, sx_of(cs_sh), sq_of(cs_sh)); }
            if constexpr (n == 154) {
                const unsigned one = (r >> 2) == g ? ((r & 1) ? 0x3f800000u : 0x3f80u) : 0u;
                const unsigned o0 = (r & 2) ? 0u : one, o1 = (r & 2) ? one : 0u, m = (wj & 1) ? ~0u : 0u;
                sel[0] = u32x4{o0, o1, 0u, 0u};
                sel[1] = u32x4{0u, 0u, o0, o1};
                sel[2] = u32x4{o0 & ~m, o1 & ~m, o0 & m, o1 & m};
                u32x4* ss = sel;
                asm volatile("" : "+v"(ss[0]), "+v"(ss[1]), "+v"(ss[2]));
            }
            if constexpr (n == 155) add2(A[0], A[1], sel[2], sel[2], sd.x[0], sd.x[1]);
            if constexpr (n == N_ADD) {
                u32x4* pp = pv;
                asm volatile("s_waitcnt vmcnt(%3)" : "+v"(pp[3]), "+v"(pp[4]), "+v"(pp[5]) : "n"(SCH.endwait));
                if (__builtin_expect(__builtin_amdgcn_ballot_w64(max(max(umax4(pv[3]), umax4(pv[4])), umax4(pv[5])) == 0xffffffffu) != 0, 0)) {
#pragma unroll
                    for (int i = 3; i < 6; ++i) settle(pv[i], theirs(i), rws, vl16);
                }
            }
            sfor<1, 6>([&](auto I) { if constexpr (n == (N_ADD + I.value < 161 ? N_ADD + I.value : 161)) add2(A[0], A[1], sel[0], sel[1], pv[I.value], pv[I.value]); });
            SB();
        });
    });
    STAMP(21);
    const u32x4 ro = mk_rsrc(out + (size_t)rt * 64 * D, 64 * D * 2);
    auto mix = [&](const float* bs, const f32x4* sum, LDSP char* dst, int pitch) {
#pragma unroll
        for (int t = 0; t < 2; ++t)
            *(LDSP u32x2*)(dst + (t * 16 + r) * pitch + g * 8) =
                u32x2{pk2(bs[4 * t] + ngs[t] * sum[t][0], bs[4 * t + 1] + ngs[t] * sum[t][1]), pk2(bs[4 * t + 2] + ngs[t] * sum[t][2], bs[4 * t + 3] + ngs[t] * sum[t][3])};
    };
    u32x4 ve;
    if (w == fc) {
        f32x4 c[2] = {};
        float be[8];
        asm volatile("s_waitcnt vmcnt(1)\n\ts_nop 1" : "+v"(c[0]), "+v"(c[1]));
#pragma unroll
        for (int i = 0; i < 8; ++i) add2(c[0], c[1], sel[0], sel[1], ev[i], ev[i]);
        add2(c[0], c[1], sel[2], sel[2], se.x[0], se.x[1]);
#pragma unroll
        for (int e = 0; e < 4; ++e) mix_base(be[2 * e], be[2 * e + 1], se.q[e], y_at(YS, e >> 1, (e & 1) * 2), y_at(YS, e >> 1, (e & 1) * 2 + 1), __uint_as_float(gt[e >> 1]), mu[e >> 1]);
        asm volatile("" : "+v"(c[0]), "+v"(c[1]));
        LDSP char* scr = lds + 2 * HALF + 12288;
        mix(be, c, scr, 48);
        ve = *(LDSP u32x4*)(scr + (l >> 1) * 48 + (l & 1) * 16);
    }
    {
        asm volatile("s_waitcnt vmcnt(0)" : "+v"(pv[6]), "+v"(pv[7]));
        settle(pv[6], theirs(6), rws, vl16);
        settle(pv[7], theirs(7), rws, vl16);
        STAMP(22);
        if (w == fc) st16c(ve, ro, (wi * 32 + (l >> 1)) * 1152 + wj * 32 + (l & 1) * 16, cs_sh * 128);
        asm volatile("s_nop 1" : "+v"(pv[6]), "+v"(pv[7]));
#pragma unroll
        for (int i = 6; i < 8; ++i) add2(acc[1][0], acc[1][1], sel[0], sel[1], pv[i], pv[i]);
        asm volatile("s_nop 7\n\ts_nop 1" : "+v"(acc[1][0]), "+v"(acc[1][1]));
        LDSP char* scr = lds + 2 * HALF;
        mix(base, acc[1], scr + wi * 32 * 144 + wj * 32, 144);
        lds_bar();
        st16c(*(LDSP u32x4*)(scr + (tid >> 3) * 144 + (l & 7) * 16), ro, (tid >> 3) * 1152 + (l & 7) * 16, cs_own * 128);
    }
    asm volatile("k_all_code_hi:");
    DRAIN(); STAMP(23); DUMP(16, 24);
}

extern "C" void kernel_launch(void* const* d_in, const int* in_sizes, int n_in, void* d_out, int out_size, void* d_ws, size_t ws_size, hipStream_t stream) {
    (void)hipFuncSetAttribute((const void*)k_all, hipFuncAttributeMaxDynamicSharedMemorySize, 163840);
    k_all<<<256, 512, 163840, stream>>>((const u16*)d_in[0], (const u16*)d_in[1], (const u16*)d_in[2], (const u16*)d_in[3], (const u16*)d_in[4], (const u16*)d_in[5],
                                        (const u16*)d_in[7], (const u16*)d_in[8], (const u16*)d_in[9], (const u16*)d_in[10], (const u16*)d_in[11],
                                        (char*)d_ws, (u16*)d_out);
}
```

```cpp
#include <hip/hip_runtime.h>
#include <hip/hip_bf16.h>
#include <stdint.h>
#include <type_traits>

typedef unsigned short u16;
typedef __attribute__((ext_vector_type(4))) short s16x4;
typedef __attribute__((ext_vector_type(8))) __bf16 bf16x8;
typedef __attribute__((ext_vector_type(2))) __bf16 bf16x2;
typedef __attribute__((ext_vector_type(4))) float f32x4;
typedef __attribute__((ext_vector_type(2))) float f32x2;
typedef __attribute__((ext_vector_type(4))) unsigned u32x4;
typedef __attribute__((ext_vector_type(2))) unsigned u32x2;
#define LDSP __attribute__((address_space(3)))
#define DI __device__ __forceinline__
#define SB() __builtin_amdgcn_sched_barrier(0)
#define IC(v) std::integral_constant<int, (v)>{}

constexpr int S = 2048, D = 576, F = 4608;
constexpr float GS = 0.46866869f;
constexpr float GALPHA = -4.91222954f;
constexpr int SPIN = 1 << 16;
constexpr float GINV = 2.13370343f;

#define WREG 2

template <int I, int N, class Fn> DI void sfor(Fn&& f) {
    if constexpr (I < N) { f(std::integral_constant<int, I>{}); sfor<I + 1, N>(f); }
}
DI float bflo(unsigned v) { return __uint_as_float(v << 16); }
DI float bfhi(unsigned v) { return __uint_as_float(v & 0xffff0000u); }
DI unsigned pk2(float a, float b) { f32x2 v = {a, b}; return __builtin_bit_cast(unsigned, __builtin_convertvector(v, bf16x2)); }
DI float rbf(float f) { return bflo(pk2(f, 0.f)); }
DI float inv_std(float var) { return __builtin_amdgcn_rcpf(rbf(__builtin_amdgcn_sqrtf(rbf(rbf(var) + 1e-5f)))); }
DI int wrap9(int v) { return v >= 9 ? v - 9 : v; }
DI u32x4 mk_rsrc(const void* p, unsigned bytes) {
    unsigned long long a = (unsigned long long)p;
    u32x4 r = {(unsigned)a, (unsigned)(a >> 32) & 0xffffu, bytes, 0x00020000u};
    return r;
}
DI void dma16(u32x4 rs, unsigned lds, unsigned voff, unsigned soff) {
    asm volatile("s_mov_b32 m0, %0\n\ts_nop 0\n\tbuffer_load_dwordx4 %1, %2, %3 offen lds" :: "s"(lds), "v"(voff), "s"(rs), "s"(soff) : "memory");
}
DI void dma16c(u32x4 rs, unsigned lds, unsigned voff, unsigned soff) {
    asm volatile("s_mov_b32 m0, %0\n\ts_nop 0\n\tbuffer_load_dwordx4 %1, %2, %3 offen sc1 lds" :: "s"(lds), "v"(voff), "s"(rs), "s"(soff) : "memory");
}
DI void dma16n(u32x4 rs, unsigned lds, unsigned voff, unsigned soff) {
    asm volatile("s_mov_b32 m0, %0\n\ts_nop 0\n\tbuffer_load_dwordx4 %1, %2, %3 offen nt lds" :: "s"(lds), "v"(voff), "s"(rs), "s"(soff) : "memory");
}
DI void ld16(u32x4& d, u32x4 rs, unsigned vo, unsigned so) { asm volatile("buffer_load_dwordx4 %0, %1, %2, %3 offen" : "=v"(d) : "v"(vo), "s"(rs), "s"(so)); }
DI void ld16c(u32x4& d, u32x4 rs, unsigned vo, unsigned so) { asm volatile("buffer_load_dwordx4 %0, %1, %2, %3 offen sc1" : "=v"(d) : "v"(vo), "s"(rs), "s"(so)); }
DI void ld4c(unsigned& d, u32x4 rs, unsigned vo, unsigned so) { asm volatile("buffer_load_dword %0, %1, %2, %3 offen sc1" : "=v"(d) : "v"(vo), "s"(rs), "s"(so)); }
DI void ld4p(unsigned& d, u32x4 rs, unsigned vo) { asm volatile("buffer_load_dword %0, %1, %2, 0 offen" : "=v"(d) : "v"(vo), "s"(rs)); }
DI void ld2c(unsigned& d, u32x4 rs, unsigned vo, unsigned so) { asm volatile("buffer_load_ushort %0, %1, %2, %3 offen sc1" : "=v"(d) : "v"(vo), "s"(rs), "s"(so)); }
DI void st16c(u32x4 v, u32x4 rs, unsigned vo, unsigned so) { asm volatile("buffer_store_dwordx4 %0, %1, %2, %3 offen sc1\n\ts_nop 1" :: "v"(v), "v"(vo), "s"(rs), "s"(so) : "memory"); }
DI void st8c(u32x2 v, u32x4 rs, unsigned vo, unsigned so) { asm volatile("buffer_store_dwordx2 %0, %1, %2, %3 offen sc1" :: "v"(v), "v"(vo), "s"(rs), "s"(so) : "memory"); }
DI void st4c(unsigned v, u32x4 rs, unsigned vo, unsigned so) { asm volatile("buffer_store_dword %0, %1, %2, %3 offen sc1" :: "v"(v), "v"(vo), "s"(rs), "s"(so) : "memory"); }
DI void st2c(unsigned v, u32x4 rs, unsigned vo, unsigned so) { asm volatile("buffer_store_short %0, %1, %2, %3 offen sc1" :: "v"(v), "v"(vo), "s"(rs), "s"(so) : "memory"); }
template <int N> DI void wait_vm() { asm volatile("s_waitcnt vmcnt(%0)" :: "n"(N) : "memory"); }
DI void bar() { asm volatile("s_barrier" ::: "memory"); }
template <int N> DI void wait_bar() { asm volatile("s_waitcnt vmcnt(%0)\n\ts_barrier" :: "n"(N) : "memory"); }
template <int N> DI void wait_bar_l() { asm volatile("s_waitcnt vmcnt(%0) lgkmcnt(0)\n\ts_barrier" :: "n"(N) : "memory"); }
DI void lds_bar() { asm volatile("s_waitcnt lgkmcnt(0)\n\ts_barrier" ::: "memory"); }
DI s16x4 tr4(LDSP char* a) { return __builtin_amdgcn_ds_read_tr16_b64_v4i16((LDSP s16x4*)a); }
DI bf16x8 join(s16x4 lo, s16x4 hi) { return __builtin_bit_cast(bf16x8, __builtin_shufflevector(lo, hi, 0, 1, 2, 3, 4, 5, 6, 7)); }
DI bf16x8 ld_tr(LDSP char* a, int second) { const s16x4 lo = tr4(a), hi = tr4(a + second); return join(lo, hi); }
DI void pin(unsigned& v) { asm volatile("" : "+v"(v)); }
DI LDSP char* opaque(LDSP char* p) { asm volatile("" : "+v"(p)); return p; }
DI f32x4 mfma(bf16x8 a, bf16x8 b, f32x4 c) { return __builtin_amdgcn_mfma_f32_16x16x32_bf16(a, b, c, 0, 0, 0); }
template <bool FIRST> DI void mfma1(f32x4& c0, const bf16x8& a, const bf16x8& b0) {
    if constexpr (FIRST) asm volatile("v_mfma_f32_16x16x32_bf16 %0, %1, %2, 0" : "=&v"(c0) : "v"(a), "v"(b0));
    else asm volatile("v_mfma_f32_16x16x32_bf16 %0, %1, %2, %0" : "+v"(c0) : "v"(a), "v"(b0));
}
DI float sum_g(float v) {
    const auto a = __builtin_amdgcn_permlane16_swap(__float_as_uint(v), __float_as_uint(v), false, false);
    v = __uint_as_float(a[0]) + __uint_as_float(a[1]);
    const auto c = __builtin_amdgcn_permlane32_swap(__float_as_uint(v), __float_as_uint(v), false, false);
    return __uint_as_float(c[0]) + __uint_as_float(c[1]);
}
DI float pick4(const f32x4& v, int i) { return i == 0 ? v[0] : i == 1 ? v[1] : i == 2 ? v[2] : v[3]; }
DI unsigned umax4(const u32x4& v) { return max(max(v[0], v[1]), max(v[2], v[3])); }
#define STAMP(i) do {} while (0)
#define DUMP(lo, n) do {} while (0)
#define DECL do {} while (0)
#define DRAIN() do {} while (0)

struct GeluT { float t0, t2, u[8]; };
template <int e> DI void gelu_blk(GeluT& T, const f32x4* P, const float* rs, unsigned* Go) {
    float t1;
    if constexpr (e == 0)
        asm volatile("v_mul_f32 %1, %3, %5\n\tv_mul_f32 %2, %4, %5\n\tv_fma_f32 %0, -%1, %1, %6\n\ts_nop 0\n\tv_mul_f32 %0, %0, %1\n\ts_nop 0\n\tv_exp_f32 %0, %0"
                     : "=&v"(T.t0), "=&v"(T.u[0]), "=&v"(T.u[1]) : "v"(P[0][0]), "v"(P[0][1]), "v"(rs[0]), "s"(GALPHA));
    else if constexpr (e < 7 && (e & 1))
        asm volatile("v_add_f32 %1, 1.0, %0\n\tv_fma_f32 %0, -%3, %3, %7\n\tv_rcp_f32 %1, %1\n\tv_mul_f32 %0, %0, %3\n\tv_mul_f32 %2, %5, %6\n\tv_exp_f32 %0, %0\n\tv_mul_f32 %1, %1, %4"
                     : "+v"(T.t0), "=&v"(T.t2), "=&v"(T.u[e + 1]) : "v"(T.u[e]), "v"(T.u[e - 1]), "v"(P[(e + 1) >> 2][(e + 1) & 3]), "v"(rs[(e + 1) >> 2]), "s"(GALPHA));
    else if constexpr (e < 7)
        asm volatile("v_add_f32 %1, 1.0, %0\n\tv_fma_f32 %0, -%4, %4, %8\n\tv_rcp_f32 %1, %1\n\tv_mul_f32 %0, %0, %4\n\tv_mul_f32 %2, %6, %7\n\tv_mul_f32 %1, %1, %5\n\tv_exp_f32 %0, %0\n\t"
                     "v_cvt_pk_bf16_f32 %3, %9, %1"
                     : "+v"(T.t0), "=&v"(t1), "=&v"(T.u[e + 1]), "=v"(Go[(e - 1) >> 1])
                     : "v"(T.u[e]), "v"(T.u[e - 1]), "v"(P[(e + 1) >> 2][(e + 1) & 3]), "v"(rs[(e + 1) >> 2]), "s"(GALPHA), "v"(T.t2));
    else if constexpr (e == 7)
        asm volatile("v_add_f32 %1, 1.0, %0\n\tv_fma_f32 %0, -%2, %2, %4\n\tv_rcp_f32 %1, %1\n\tv_mul_f32 %0, %0, %2\n\tv_mul_f32 %1, %1, %3\n\tv_exp_f32 %0, %0"
                     : "+v"(T.t0), "=&v"(T.t2) : "v"(T.u[7]), "v"(T.u[6]), "s"(GALPHA));
    else
        asm volatile("v_add_f32 %0, 1.0, %2\n\ts_nop 0\n\tv_rcp_f32 %0, %0\n\ts_nop 0\n\tv_mul_f32 %0, %0, %3\n\ts_nop 0\n\tv_cvt_pk_bf16_f32 %1, %4, %0"
                     : "=&v"(t1), "=v"(Go[3]) : "v"(T.t0), "v"(T.u[7]), "v"(T.t2));
}
DI unsigned gelu2(float v0, float v1, float rs) {
    float a, b, u0, u1; unsigned o;
    asm volatile("v_mul_f32 %3, %5, %7\n\tv_mul_f32 %4, %6, %7\n\t"
                 "v_fma_f32 %0, -%3, %3, %8\n\tv_fma_f32 %1, -%4, %4, %8\n\tv_mul_f32 %0, %0, %3\n\tv_mul_f32 %1, %1, %4\n\tv_exp_f32 %0, %0\n\tv_exp_f32 %1, %1\n\t"
                 "v_add_f32 %0, 1.0, %0\n\tv_add_f32 %1, 1.0, %1\n\tv_rcp_f32 %0, %0\n\tv_rcp_f32 %1, %1\n\tv_mul_f32 %0, %0, %3\n\tv_mul_f32 %1, %1, %4\n\ts_nop 0\n\t"
                 "v_cvt_pk_bf16_f32 %2, %0, %1" : "=&v"(a), "=&v"(b), "=v"(o), "=&v"(u0), "=&v"(u1) : "v"(v0), "v"(v1), "v"(rs), "s"(GALPHA));
    return o;
}

constexpr int HALF = 73728;
#define XPRE 6
#define NB3 99
#define XNT 1
#define NL 156
#define FILLS 16, 25, 34, 43, 52
#define NCHK 129
#define NSH 156
#define BX 2
#define NQ 79
#define KB2 ((cb & 1) * 4)
#define QN 13
#define B1 5
#define B2 3
#define L1 12
#define L2 9
constexpr bool is_bar(int n) {
    return n <= 15 ? n % 3 == 0 : n <= 81 ? (n > 17 && (81 - n) % B1 == 0) : n <= 89 ? (n - 81) % BX == 0 : n <= NB3 ? n % 3 == 0 : n <= NL ? (n > NB3 + 2 && (NL - n) % B2 == 0) : false;
}
constexpr int bar_from(int n) { while (!is_bar(n)) ++n; return n; }
#define GETS 130, 134, 138, 142, 146, 154, 160
#define NADD 155
constexpr int N_YCHK = bar_from(62), N_YPOLL = N_YCHK - 7, N_PUB = bar_from(113), N_CHK = bar_from(NCHK), N_POLL = N_CHK - 5, N_ADD = NADD;
constexpr int fill_at(int n) { constexpr int t[5] = {FILLS}; for (int i = 0; i < 5; ++i) if (t[i] == n) return 3 + i; return -1; }
constexpr int get_n(int i) { constexpr int t[7] = {GETS}; return t[i - 1]; }
constexpr int n_get(int n) { int c = 0; for (int i = 1; i < 8; ++i) c += get_n(i) == n; return c; }
constexpr int n_store(int n) { return n >= 90 && n % 9 == 0; }
struct Sched { int d1[162], d2[162], vm[162]; int ywait, pollwait, endwait; };
constexpr Sched make_sched() {
    Sched s{};
    int next = 9, a = 0;
    for (int n = 0; n < 162; ++n) {
        if (is_bar(n)) a = n;
        s.d1[n] = s.d2[n] = -1;
        for (int c = 0; c < 2; ++c) {
            const int m = next;
            const int freed = m < 18 ? m - 9 : (m >= 90 && m < 99) ? m - 9 : m - 18;
            if (m > 161 || a <= freed || (n >= NQ && n < 81) || m - n > (n < 81 ? L1 : L2)) break;
            if (c == 1 && n < 81 && (n & 1)) break;
            (c ? s.d2[n] : s.d1[n]) = m; ++next;
        }
    }
    int pos_dma[162] = {}, before[163] = {}, pos_st3 = 0, pos_get3 = 0;
    int p = 0;
    for (int n = 0; n < 9; ++n) pos_dma[n] = p++;
    p += XPRE;
    for (int n = 0; n < 162; ++n) {
        before[n] = p;
        if (n == 0) p += 12 - XPRE;
        if (n == 81) p += 6;
        if (s.d1[n] >= 0) pos_dma[s.d1[n]] = p++;
        if (s.d2[n] >= 0) pos_dma[s.d2[n]] = p++;
        if (n == 108) pos_st3 = p;
        p += n_store(n);
        p += (fill_at(n) >= 0) + n_get(n);
        if (get_n(5) == n) pos_get3 = p - 1;
    }
    for (int n = 0; n < 162; ++n) {
        s.vm[n] = -1;
        if (!is_bar(n)) continue;
        int last = pos_dma[n == NL ? 161 : bar_from(n + 1)];
        if (n < 6) last = 9 + n + 3;
        if (n == N_PUB && pos_st3 > last) last = pos_st3;
        if (last > 0 && last < before[n]) s.vm[n] = before[n] - 1 - last;
    }
    s.ywait = before[N_YCHK] - before[N_YPOLL];
    s.pollwait = before[N_CHK] - before[N_POLL];
    s.endwait = before[N_ADD] - 1 - pos_get3;
    return s;
}
constexpr Sched SCH = make_sched();

struct Side { u32x4 x[2], q; };
DI void mix_base(float& b0, float& b1, unsigned qq, unsigned y0, unsigned y1, float gv, float c) {
    float t0, t1;
    asm volatile("v_lshlrev_b32 %0, 16, %4\n\tv_and_b32 %1, 0xffff0000, %4\n\tv_lshlrev_b32 %2, 16, %5\n\tv_lshlrev_b32 %3, 16, %6\n\t"
                 "v_mul_f32 %0, %0, %2\n\tv_mul_f32 %1, %1, %3\n\tv_fma_f32 %0, %0, %7, %8\n\tv_fma_f32 %1, %1, %7, %8"
                 : "=&v"(b0), "=&v"(b1), "=&v"(t0), "=&v"(t1) : "v"(qq), "v"(y0), "v"(y1), "v"(gv), "v"(c));
}
DI void add2(f32x4& c0, f32x4& c1, const u32x4& s0, const u32x4& s1, const u32x4& v0, const u32x4& v1) {
    asm volatile("v_mfma_f32_16x16x32_bf16 %0, %2, %4, %0\n\tv_mfma_f32_16x16x32_bf16 %1, %3, %5, %1" : "+v"(c0), "+v"(c1) : "v"(s0), "v"(s1), "v"(v0), "v"(v1));
}
DI void settle(u32x4& v, unsigned so, u32x4 rp, unsigned vo) {
#pragma unroll 1
    for (int it = 0; it < SPIN; ++it) {
        if (__builtin_amdgcn_ballot_w64(umax4(v) == 0xffffffffu) == 0) break;
        asm volatile("buffer_load_dwordx4 %0, %1, %2, %3 offen sc1\n\ts_waitcnt vmcnt(0)" : "=&v"(v) : "v"(vo), "s"(rp), "s"(so) : "memory");
    }
}
DI void ld_shared(u32x4* ev, Side& se, int skip, u32x4 rs, unsigned vo, unsigned so, unsigned sx, unsigned sq) {
    unsigned t;
    asm volatile("s_cmp_lg_u32 %12, 0\n\ts_cbranch_scc1 1f\n\t"
                 "buffer_load_dwordx4 %0, %14, %13, %15 offen sc1\n\ts_add_u32 %11, %15, 0x2000\n\t"
                 "buffer_load_dwordx4 %1, %14, %13, %11 offen sc1\n\ts_add_u32 %11, %15, 0x4000\n\t"
                 "buffer_load_dwordx4 %2, %14, %13, %11 offen sc1\n\ts_add_u32 %11, %15, 0x6000\n\t"
                 "buffer_load_dwordx4 %3, %14, %13, %11 offen sc1\n\ts_add_u32 %11, %15, 0x8000\n\t"
                 "buffer_load_dwordx4 %4, %14, %13, %11 offen sc1\n\ts_add_u32 %11, %15, 0xa000\n\t"
                 "buffer_load_dwordx4 %5, %14, %13, %11 offen sc1\n\ts_add_u32 %11, %15, 0xc000\n\t"
                 "buffer_load_dwordx4 %6, %14, %13, %11 offen sc1\n\ts_add_u32 %11, %15, 0xe000\n\t"
                 "buffer_load_dwordx4 %7, %14, %13, %11 offen sc1\n\t"
                 "buffer_load_dwordx4 %8, %14, %13, %16 offen sc1\n\t"
                 "buffer_load_dwordx4 %9, %14, %13, %16 offen offset:2048 sc1\n\t"
                 "buffer_load_dwordx4 %10, %14, %13, %17 offen sc1\n\t"
                 "1:"
                 : "=&v"(ev[0]), "=&v"(ev[1]), "=&v"(ev[2]), "=&v"(ev[3]), "=&v"(ev[4]), "=&v"(ev[5]), "=&v"(ev[6]), "=&v"(ev[7]),
                   "=&v"(se.x[0]), "=&v"(se.x[1]), "=&v"(se.q), "=&s"(t)
                 : "s"(skip), "s"(rs), "v"(vo), "s"(so), "s"(sx), "s"(sq) : "scc");
}
DI void dma16c_if(int skip, u32x4 rs, unsigned lds, unsigned voff, unsigned soff) {
    asm volatile("s_cmp_lg_u32 %4, 0\n\ts_cbranch_scc1 1f\n\ts_mov_b32 m0, %0\n\ts_nop 0\n\tbuffer_load_dwordx4 %1, %2, %3 offen sc1 lds\n\t1:"
                 :: "s"(lds), "v"(voff), "s"(rs), "s"(soff), "s"(skip) : "scc", "memory");
}
DI void ld_flag(unsigned& v, int skip, u32x4 rs, unsigned vf) {
    asm volatile("s_cmp_lg_u32 %1, 0\n\ts_cbranch_scc1 1f\n\tbuffer_load_dword %0, %3, %2, 0 offen sc1\n\t1:" : "=&v"(v) : "s"(skip), "s"(rs), "v"(vf) : "scc");
}
template <int N> DI void chk_flag(unsigned& v, int skip, unsigned tag, u32x4 rs, unsigned vf) {
    unsigned c;
    asm volatile("s_cmp_lg_u32 %2, 0\n\ts_cbranch_scc1 2f\n\ts_waitcnt vmcnt(%6)\n\ts_mov_b32 %1, 0x10000\n\t"
                 "1:\n\tv_cmp_ne_u32 vcc, %3, %0\n\ts_cbranch_vccz 2f\n\ts_sleep 1\n\tbuffer_load_dword %0, %5, %4, 0 offen sc1\n\ts_waitcnt vmcnt(0)\n\t"
                 "s_sub_u32 %1, %1, 1\n\ts_cbranch_scc0 1b\n\t2:"
                 : "+v"(v), "=&s"(c) : "s"(skip), "s"(tag), "s"(rs), "v"(vf), "n"(N) : "scc", "vcc", "memory");
}
DI void st_flag(unsigned tag, int skip, u32x4 rs, unsigned so) {
    unsigned t;
    asm volatile("s_cmp_lg_u32 %1, 0\n\ts_cbranch_scc1 1f\n\tv_mov_b32 %0, %2\n\tbuffer_store_dword %0, off, %3, %4 sc1\n\t1:" : "=&v"(t) : "s"(skip), "s"(tag), "s"(rs), "s"(so) : "scc", "memory");
}
DI void await(unsigned v, unsigned tag, u32x4 rs, unsigned vf) {
#pragma unroll 1
    for (int it = 0; it < SPIN; ++it) {
        if (__builtin_amdgcn_ballot_w64(v != tag) == 0) break;
        if (it) __builtin_amdgcn_s_sleep(1);
        asm volatile("buffer_load_dword %0, %1, %2, 0 offen sc1\n\ts_waitcnt vmcnt(0)" : "=v"(v) : "v"(vf), "s"(rs) : "memory");
    }
}
DI void sld3(unsigned& a, u32x4& b4, unsigned& c, const void* pa, const void* pb, const void* pc) {
    asm volatile("s_load_dword %0, %3, 0x0\n\ts_load_dwordx4 %1, %4, 0x0\n\ts_load_dword %2, %5, 0x0\n\ts_waitcnt lgkmcnt(0)"
                 : "=&s"(a), "=&s"(b4), "=&s"(c) : "s"(pa), "s"(pb), "s"(pc) : "memory");
}
template <int AUX> DI void warm(const void* base, unsigned bytes, int first, int step, int end, int pitch, int per, int col) {
    auto rs = __builtin_amdgcn_make_buffer_rsrc((void*)base, 0, bytes, 0x00020000);
    unsigned a = 0;
#pragma unroll 1
    for (int ln = first; ln < end; ln += step) a |= __builtin_amdgcn_raw_buffer_load_b32(rs, (ln / per) * pitch + col + (ln % per) * 128, 0, AUX);
    asm volatile("" :: "v"(a));
}

constexpr unsigned NB = S * D * 2;
#define TP 4096

constexpr unsigned O_FA = 4096, O_FB = O_FA + 32768, O_FP = O_FB + 32768, O_GATE = O_FP + 32768, O_RHO = O_GATE + 8192, O_STAT = O_RHO + 4096;
constexpr unsigned O_X = 524288, O_Q = O_X + NB, O_Y = O_Q + NB, O_H = O_Y + D * TP + 4096, O_U = O_H + NB, O_PART = O_U + D * TP, WS_END = O_PART + 8 * NB;
constexpr int SL = 24576, HRING = 5 * SL, AUXO = HRING + 32768;
constexpr int w2slot(int i) { return (i < 3 ? 4 : i < 6 ? 0 : 1) * SL + (i % 3) * 8192; }
constexpr int hslot(int i) { return i % 5 < 3 ? 2 * SL + (i % 5) * 16384 : HRING + (i % 5 - 3) * 16384; }
constexpr int CV_U = 6 * 16640;

__global__ __launch_bounds__(512, 2) void k_all(const u16* __restrict__ x, const u16* __restrict__ kin, const u16* __restrict__ vin, const u16* __restrict__ shk,
                                                const u16* __restrict__ ssk, const u16* __restrict__ ssD, const u16* __restrict__ w1, const u16* __restrict__ w2,
                                                const u16* __restrict__ w3, const u16* __restrict__ w4, const u16* __restrict__ gwt,
                                                char* __restrict__ ws, u16* __restrict__ out) {
    extern __shared__ __attribute__((aligned(16))) char smem[];
    LDSP char* lds = (LDSP char*)smem;
    const int tid = threadIdx.x, l = tid & 63;
    const int w = __builtin_amdgcn_readfirstlane(tid >> 6);
    const int b = blockIdx.x, xc = b & 7, idx = b >> 3;
    const int g = l >> 4, q = (l >> 2) & 3, p = l & 3, r = l & 15;
    const unsigned vl16 = l * 16;
    unsigned sink;
    {
        unsigned long long pc; unsigned len;
        asm volatile("s_getpc_b64 %0\nk_all_code_lo:\n\ts_mov_b32 %1, k_all_code_hi-k_all_code_lo" : "=s"(pc), "=s"(len));
        const u32x4 rc = {(unsigned)pc, (unsigned)(pc >> 32) & 0xffffu, len, 0x00020000u};
        asm volatile("s_cmp_lg_u32 %1, 3\n\ts_cbranch_scc1 1f\n\tbuffer_load_dword %0, %3, %2, 0 offen\n\t1:" : "=&v"(sink) : "s"(w), "s"(rc), "v"((l * 32 + idx) * 128) : "scc");
        SB();
    }
    asm volatile("" : "+s"(x), "+s"(kin), "+s"(vin), "+s"(shk), "+s"(ssk), "+s"(ssD), "+s"(w1), "+s"(w2), "+s"(w3), "+s"(w4), "+s"(gwt), "+s"(ws), "+s"(out));
    const u32x4 rws = mk_rsrc(ws, WS_END);
    const int sw = ((g & 1) << 1) | (q >> 1);
    const int u = w & 3;
    unsigned tag, dD;
    u32x4 k0;
    auto scalars = [&](int c2) {
        sld3(tag, k0, dD, ws, ssk + (size_t)c2 * S, ssD + (c2 & ~1));
        tag = tag * 1664525u + 1013904223u;
        dD = (c2 & 1) ? dD >> 16 : dD & 0xffffu;
    };
    { DECL; constexpr unsigned smask = 0x421; (void)smask; STAMP(0);
    if (idx < 18) {
        const int rt = 2 * xc + idx / 9, cb = idx % 9;
        const int kb0 = wrap9(cb + (idx / 9) * 4), kb2 = KB2;
        if (w >= 4) {
            const int lrow = u * 16 + (l >> 3);
            const unsigned vx0 = lrow * 1152 + (((l & 7) ^ g) * 16), vx1 = (lrow + 8) * 1152 + (((l & 7) ^ (4 + g)) * 16);
            const unsigned vw0 = lrow * 1152 + cb * 128 + ((l & 7) ^ ((g & 1) << 1)) * 16, vw1 = (lrow + 8) * 1152 + cb * 128 + ((l & 7) ^ (((g & 1) | 2) << 1)) * 16;
            const u32x4 rx = mk_rsrc(x + (size_t)rt * 128 * D, 128 * D * 2), rw = mk_rsrc(w1, D * D * 2), rw2 = mk_rsrc(w2, D * D * 2), rh = mk_rsrc(ws + O_H - 2304, S * D * 2 + 2304);
            auto issue_x = [&](unsigned dst, int kb, bool first) {
                if (first) {
                    dma16n(rx, dst, vx0, kb * 128); dma16n(rx, dst + 1024, vx1, kb * 128);
                    dma16n(rx, dst + 8192, vx0, kb * 128 + 64 * 1152); dma16n(rx, dst + 8192 + 1024, vx1, kb * 128 + 64 * 1152);
                } else {
                    dma16(rx, dst, vx0, kb * 128); dma16(rx, dst + 1024, vx1, kb * 128);
                    dma16(rx, dst + 8192, vx0, kb * 128 + 64 * 1152); dma16(rx, dst + 8192 + 1024, vx1, kb * 128 + 64 * 1152);
                }
            };
            auto issue = [&](int i) {
                const int kb = wrap9(kb0 + i);
                const unsigned dst = (i % 5) * SL + u * 2048;
                dma16(rw, dst + 16384, vw0, kb * 73728); dma16(rw, dst + 16384 + 1024, vw1, kb * 73728);
                issue_x(dst, kb, i < XNT);
            };
            auto issue_w2 = [&](int i0) {
#pragma unroll
                for (int i = i0; i < i0 + 3; ++i) { const unsigned so = wrap9(kb2 + i) * 73728; dma16(rw2, w2slot(i) + u * 2048, vw0, so); dma16(rw2, w2slot(i) + u * 2048 + 1024, vw1, so); }
            };
            issue(0); bar(); issue(1);
            sfor<0, 7>([&](auto I) { wait_bar<6>(); issue(I.value + 2); });
            wait_bar<6>();
            issue_w2(0);
            wait_bar<6>();
            issue_w2(3);
            bar();
            issue_x(2 * SL + u * 2048, cb, false);
            issue_w2(6);
            scalars(0);
            wait_bar<6>();
            unsigned vh[9][2], vfh;
            {
                const int t = min(max(rt + (l < 9 ? 0 : l < 15 ? -1 : 1), 0), 15);
                const int j = l < 9 ? l : l < 15 ? l - 9 + (l >= 12) : l - 13 + (l >= 18);
                const int n = min(l - 21, 35);
                vfh = O_FA + (l < 21 ? ((t & 1) * 9 + j) * 8 + (t >> 1) : (27 + n % 5) * 8 + n / 5) * 128;
                pin(vfh);
            }
            unsigned f0;
            ld_flag(f0, w - 4, rws, vfh);
#pragma unroll
            for (int e = 0; e < 2; ++e) {
                const int c = (l & 7) ^ (4 * e + g);
                const unsigned v0 = e ? vx1 : vx0, v2 = v0 + (c < 2 ? 0 : 2 * 1152), v4 = v0 + (c < 4 ? 3 * 1152 : 0), v6 = v0 + (c < 6 ? 0 : 4 * 1152);
#pragma unroll
                for (int i = 0; i < 9; ++i) { const int kb = wrap9(kb2 + i); vh[i][e] = kb == 2 ? v2 : kb == 4 ? v4 : kb == 6 ? v6 : v0; pin(vh[i][e]); }
            }
            chk_flag<0>(f0, w - 4, tag, rws, vfh);
            wait_bar<0>();
            auto issue_h = [&](int i) {
                const int kb = wrap9(kb2 + i);
                const unsigned so = (rt * 128 + (kb < 3 ? 1 : kb == 3 ? 3 : kb < 7 ? 0 : 4)) * 1152 + kb * 128;
#pragma unroll
                for (int e = 0; e < 4; ++e) dma16c(rh, hslot(i) + (e >> 1) * 8192 + u * 2048 + (e & 1) * 1024, vh[i][e & 1], so + (e >> 1) * 64 * 1152);
            };
            dma16c(rws, AUXO + 4608 + min(u, 2) * 1024, vl16, O_RHO + min(u, 2) * 1024);
            issue_h(0); issue_h(1);
            sfor<0, 9>([&](auto I) {
                constexpr int i = I.value;
                wait_bar<(i == 0 ? 4 : i < 7 ? 8 : 4 * (8 - i))>();
                if constexpr (i == 0) issue_h(2);
                if constexpr (i + 3 < 9) issue_h(i + 3);
            });
        } else {
            if (w < 2) dma16(mk_rsrc(gwt, D * 2), AUXO + w * 1024, vl16, w * 1024);
            bar();
            scalars(0);
            if (tid < 20) *(LDSP unsigned*)(lds + AUXO + 2048 + tid * 4) = 0x3f803f80u;
            float mean[2];
            LDSP char* xa[2];
#pragma unroll
            for (int s2 = 0; s2 < 2; ++s2) xa[s2] = lds + (w * 32 + r) * 128 + (((4 * s2 + g) ^ (r >> 1)) * 16);
            {
                LDSP char* xb[2][2]; LDSP char* wb[2][4];
#pragma unroll
                for (int hi = 0; hi < 2; ++hi) {
#pragma unroll
                    for (int s2 = 0; s2 < 2; ++s2) xb[hi][s2] = opaque(xa[s2] + hi * 2 * SL);
#pragma unroll
                    for (int c = 0; c < 4; ++c) wb[hi][c] = opaque(lds + hi * 2 * SL + 16384 + (8 * g + q) * 128 + (((c + w) & 3) ^ sw) * 32 + p * 8);
                }
                LDSP char* ob[9];
#pragma unroll
                for (int i = 0; i < 9; ++i) ob[i] = opaque(lds + AUXO + (r == 1 ? g * 16 + wrap9(kb0 + i) * 128 : 2048));
                u32x4 on = {0x3f803f80u, 0x3f803f80u, 0x3f803f80u, 0x3f803f80u};
                LDSP float* st = (LDSP float*)(lds + AUXO + 4096);
                LDSP char* str_[4]; LDSP char* hwp[4];
                LDSP char* hw = lds + HRING + w * 32 * 144;
#pragma unroll
                for (int c = 0; c < 4; ++c) { str_[c] = opaque((LDSP char*)(st + ((c + w) & 3) * 16 + g * 4)); hwp[c] = opaque(hw + r * 144 + ((c + w) & 3) * 32 + g * 8); }
                LDSP char* hrp = opaque(hw + (l >> 3) * 144 + (l & 7) * 16);
                unsigned vho = (l >> 3) * 1152 + (l & 7) * 16, vdg = ((r >> 2) * 16 + r) * 4, sho = O_H + (rt * 128 + w * 32) * 1152 + cb * 128;
                asm volatile("" : "+v"(vho), "+v"(vdg), "+v"(on));
                asm volatile("" : "+s"(sho));
                f32x4 acc[2][4] = {}, a_sum[2] = {}, a_sq[2] = {}, a_cs = {};
                asm volatile("s_waitcnt vmcnt(0)" : "+v"(acc[0][0]), "+v"(acc[0][1]), "+v"(acc[0][2]), "+v"(acc[0][3]), "+v"(acc[1][0]), "+v"(acc[1][1]), "+v"(acc[1][2]), "+v"(acc[1][3]),
                             "+v"(a_sum[0]), "+v"(a_sum[1]), "+v"(a_sq[0]), "+v"(a_sq[1]), "+v"(a_cs));
                bf16x8 xf[2][2], of[2];
                s16x4 wh[2][4][2];
                float sq[2], gd;
                sfor<0, 19>([&](auto KS) {
                    constexpr int n = KS.value, i = n >> 1, s2 = n & 1, nb = n & 1, a = nb ^ 1;
                    constexpr int sl = i % 5, hi = sl >= 2, off = (sl - 2 * hi) * SL;
                    if constexpr (s2 == 0 && n < 18) {
                        if constexpr (i == 0) lds_bar(); else bar();
                        if constexpr (i == 0) STAMP(1);
                        if constexpr (i == 8) STAMP(2);
                    }
                    auto rd = [&](int e) {
                        if constexpr (n < 18) {
                            if (e < 2) xf[nb][e] = *(LDSP bf16x8*)(xb[hi][s2] + off + e * 2048);
                            else if (e == 2) of[nb] = *(LDSP bf16x8*)(ob[i] + s2 * 64);
                            else if (e < 11) wh[nb][(e - 3) >> 1][(e - 3) & 1] = tr4(wb[hi][(e - 3) >> 1] + off + s2 * 4096 + ((e - 3) & 1) * 512);
                        } else {
                            if (e == 8) {
                                f32x4& c0 = a_cs; f32x4* c1 = a_sq; f32x4* c2 = a_sum;
                                asm volatile("" : "+v"(c0), "+v"(c1[0]), "+v"(c1[1]), "+v"(c2[1]));
                                if (r == 0) *(LDSP f32x4*)str_[0] = a_cs;
                            }
                            if (e == 9) sq[0] = __int_as_float(__builtin_amdgcn_ds_bpermute(vdg, __float_as_int(pick4(a_sq[0], r & 3))));
                            if (e == 10) sq[1] = __int_as_float(__builtin_amdgcn_ds_bpermute(vdg, __float_as_int(pick4(a_sq[1], r & 3))));
                            if (e == 11) gd = __int_as_float(__builtin_amdgcn_ds_bpermute(r * 4, __float_as_int(a_sum[1][1])));
                        }
                        SB();
                    };
#pragma unroll
                    for (int m = 0; m < 13; ++m) {
                        if constexpr (n >= 1) {
                            if (m < 2) mfma1<false>(a_sq[m], xf[a][m], xf[a][m]);
                            else if (m < 4) mfma1<false>(a_sum[m - 2], of[a], xf[a][m - 2]);
                            else if (m == 4) mfma1<false>(a_cs, join(wh[a][0][0], wh[a][0][1]), __builtin_bit_cast(bf16x8, on));
                            else mfma1<false>(acc[(m - 5) & 1][(m - 5) >> 1], join(wh[a][(m - 5) >> 1][0], wh[a][(m - 5) >> 1][1]), xf[a][(m - 5) & 1]);
                            if (n == 18 && m == 8) asm volatile("s_nop 4");
                            SB();
                        }
                        rd(m);
                    }
                });
                lds_bar();
                STAMP(3);
                f32x4 cs[4];
#pragma unroll
                for (int c = 0; c < 4; ++c) cs[c] = *(LDSP f32x4*)str_[c];
                asm volatile("s_nop 4" : "+v"(acc[0][0]), "+v"(acc[0][1]), "+v"(acc[0][2]), "+v"(acc[0][3]), "+v"(acc[1][0]), "+v"(acc[1][1]), "+v"(acc[1][2]), "+v"(acc[1][3]),
                             "+v"(a_sum[0]), "+v"(gd));
                float mr[2], rs[2];
#pragma unroll
                for (int t = 0; t < 2; ++t) {
                    mean[t] = a_sum[t][0] * (1.f / D);
                    rs[t] = GS * inv_std(fmaxf(sq[t] * (1.f / D) - mean[t] * mean[t], 0.f));
                    mr[t] = -rbf(mean[t]) * rs[t];
                }
                const f32x2 al = {GALPHA, GALPHA};
                u32x4 o[4];
                SB();
#pragma unroll
                for (int t = 0; t < 2; ++t) {
                    f32x2 uu[8], z[8];
#pragma unroll
                    for (int e = 0; e < 8; ++e) uu[e] = f32x2{cs[e >> 1][(e & 1) * 2], cs[e >> 1][(e & 1) * 2 + 1]} * mr[t];
                    SB();
#pragma unroll
                    for (int e = 0; e < 8; ++e) uu[e] = __builtin_elementwise_fma(f32x2{acc[t][e >> 1][(e & 1) * 2], acc[t][e >> 1][(e & 1) * 2 + 1]}, f32x2{rs[t], rs[t]}, uu[e]);
                    SB();
#pragma unroll
                    for (int e = 0; e < 8; ++e) z[e] = __builtin_elementwise_fma(-uu[e], uu[e], al);
                    SB();
#pragma unroll
                    for (int e = 0; e < 8; ++e) z[e] *= uu[e];
                    SB();
                    if (t == 1) { st16c(o[0], rws, vho, sho); st16c(o[1], rws, vho, sho + 8 * 1152); }
#pragma unroll
                    for (int e = 0; e < 8; ++e) z[e] = f32x2{__builtin_amdgcn_exp2f(z[e][0]), __builtin_amdgcn_exp2f(z[e][1])};
                    SB();
#pragma unroll
                    for (int e = 0; e < 8; ++e) z[e] += 1.f;
                    SB();
#pragma unroll
                    for (int e = 0; e < 8; ++e) z[e] = f32x2{__builtin_amdgcn_rcpf(z[e][0]), __builtin_amdgcn_rcpf(z[e][1])};
                    SB();
#pragma unroll
                    for (int e = 0; e < 8; ++e) z[e] *= uu[e];
                    SB();
#pragma unroll
                    for (int c = 0; c < 4; ++c) *(LDSP u32x2*)(hwp[c] + t * 16 * 144) = u32x2{pk2(z[2 * c][0], z[2 * c][1]), pk2(z[2 * c + 1][0], z[2 * c + 1][1])};
                    o[2 * t] = *(LDSP u32x4*)(hrp + t * 16 * 144); o[2 * t + 1] = *(LDSP u32x4*)(hrp + (t * 16 + 8) * 144);
                    SB();
                }
                st16c(o[2], rws, vho, sho + 16 * 1152); st16c(o[3], rws, vho, sho + 24 * 1152);
                if (cb == 0 && g < 2) st4c(__float_as_uint(rbf(__builtin_amdgcn_rcpf(1.f + __builtin_amdgcn_exp2f(-1.44269504f * rbf(g ? gd : a_sum[0][1]))))), rws, l * 4, O_GATE + (rt * 128 + w * 32) * 4);
                STAMP(4);
            }
            LDSP char* xb[2][2]; LDSP char* wb[2][4];
#pragma unroll
            for (int hi = 0; hi < 2; ++hi) {
#pragma unroll
                for (int s2 = 0; s2 < 2; ++s2) xb[hi][s2] = opaque(xa[s2] + (hi ? HRING : 2 * SL));
#pragma unroll
                for (int c = 0; c < 4; ++c) wb[hi][c] = opaque(lds + hi * 4 * SL + (8 * g + q) * 128 + (c ^ sw) * 32 + p * 8);
            }
            LDSP char* ob = opaque(lds + AUXO + 4608 + (r & 1) * 1152 + g * 16 + kb2 * 128);
            LDSP char* ob2 = opaque(ob - 1152);
            unsigned sxo = O_X + ((rt * 2 + (w >> 1)) * 9 + cb) * 8192 + (w & 1) * 4096, sso = O_STAT + ((rt * 4 + w) * 9 + cb) * 256;
            unsigned sqo = O_Q + ((rt * 4 + w) * 36 + cb * 4) * 1024;
            asm volatile("" : "+s"(sxo), "+s"(sso), "+s"(sqo));
            wait_bar<0>();
            if (tid == 0) st4c(tag, rws, 0, O_FA + b * 128);
            STAMP(5);
            f32x4 acc[2][4], a_m[2] = {};
            u32x4 qo[4];
#pragma unroll
            for (int c = 0; c < 4; ++c) {
                u32x2 xv[2];
#pragma unroll
                for (int t = 0; t < 2; ++t) {
                    xv[t] = *(LDSP u32x2*)(lds + 2 * SL + (w * 32 + t * 16 + r) * 128 + (((c * 2 + (g >> 1)) ^ (r >> 1)) * 16) + (g & 1) * 8);
                    acc[t][c] = (f32x4{bflo(xv[t][0]), bfhi(xv[t][0]), bflo(xv[t][1]), bfhi(xv[t][1])} - mean[t]) * GS;
                }
                qo[c] = u32x4{xv[0][0], xv[0][1], xv[1][0], xv[1][1]};
            }
            asm volatile("" : "+v"(acc[0][0]), "+v"(acc[0][1]), "+v"(acc[0][2]), "+v"(acc[0][3]), "+v"(acc[1][0]), "+v"(acc[1][1]), "+v"(acc[1][2]), "+v"(acc[1][3]), "+v"(a_m[0]), "+v"(a_m[1]));
            bf16x8 wr[18][WREG];
#pragma unroll
            for (int n = 0; n < 18; ++n) {
                if (n == 12) lds_bar();
#pragma unroll
                for (int c = 0; c < WREG; ++c) wr[n][c] = ld_tr(wb[n < 6][c] + w2slot(n >> 1) - (n < 6 ? 4 * SL : 0) + (n & 1) * 4096, 512);
            }
            STAMP(6);
            bf16x8 xf[2][2], of[2];
            s16x4 wh[2][4][2];
            sfor<0, 19>([&](auto KS) {
                constexpr int n = KS.value, i = n >> 1, s2 = n & 1, nb = n & 1, a = nb ^ 1;
                constexpr int xhi = i % 5 >= 3, xoff = hslot(i) - (xhi ? HRING : 2 * SL), whi = i < 3, woff = w2slot(i) - (whi ? 4 * SL : 0);
                if constexpr (s2 == 0 && n < 18) {
                    bar();
                    if constexpr (i == 0) STAMP(7);
                    if constexpr (i == 8) STAMP(8);
                }
                auto rd = [&](int e) {
                    if constexpr (n < 18) {
                        if (e < 2) xf[nb][e] = *(LDSP bf16x8*)(xb[xhi][s2] + xoff + e * 2048);
                        else if (e == 2) of[nb] = *(LDSP bf16x8*)((kb2 + i >= 9 ? ob2 : ob) + n * 64);
                        else if (e < 11 - 2 * WREG) { const int f = e - 3 + 2 * WREG; wh[nb][f >> 1][f & 1] = tr4(wb[whi][f >> 1] + woff + s2 * 4096 + (f & 1) * 512); }
                    }
                    SB();
                };
#pragma unroll
                for (int m = 0; m < 10; ++m) {
                    if constexpr (n >= 1) {
                        if (m < 2 * WREG) mfma1<false>(acc[m & 1][m >> 1], wr[n - 1][(m >> 1) % WREG], xf[a][m & 1]);
                        else if (m < 2 * WREG + 2) mfma1<false>(a_m[m & 1], of[a], xf[a][m & 1]);
                        else mfma1<false>(acc[m & 1][(m - 2) >> 1], join(wh[a][(m - 2) >> 1][0], wh[a][(m - 2) >> 1][1]), xf[a][m & 1]);
                        SB();
                    }
                    rd(m);
                }
                if constexpr (n >= QN && n < QN + 4) { st16c(qo[n - QN], rws, vl16, sqo + (n - QN) * 1024); SB(); }
            });
            asm volatile("s_nop 7" : "+v"(acc[0][0]), "+v"(acc[0][1]), "+v"(acc[0][2]), "+v"(acc[0][3]), "+v"(acc[1][0]), "+v"(acc[1][1]), "+v"(acc[1][2]), "+v"(acc[1][3]), "+v"(a_m[0]), "+v"(a_m[1]));
            STAMP(9);
            f32x4 s2[2] = {};
            float mh[2];
#pragma unroll
            for (int t = 0; t < 2; ++t) mh[t] = a_m[t][0] + a_m[t][1];
#pragma unroll
            for (int m = 0; m < 2; ++m)
#pragma unroll
                for (int t = 0; t < 2; ++t) {
                    const f32x4 a0 = acc[t][2 * m] - mh[t], a1 = acc[t][2 * m + 1] - mh[t];
                    const u32x4 xo = {pk2(a0[0], a0[1]), pk2(a0[2], a0[3]), pk2(a1[0], a1[1]), pk2(a1[2], a1[3])};
                    st16c(xo, rws, vl16, sxo + t * 2048 + m * 1024);
                    s2[t] = mfma(__builtin_bit_cast(bf16x8, xo), __builtin_bit_cast(bf16x8, xo), s2[t]);
                }
            const u32x4 so = {__float_as_uint(pick4(s2[0], r & 3)), __float_as_uint(pick4(s2[1], r & 3)), __float_as_uint(mean[0] * GS + mh[0]), __float_as_uint(mean[1] * GS + mh[1])};
            if (g == (r >> 2)) st16c(so, rws, r * 16, sso);
            STAMP(10);
        }
    } else {
        const int j = idx - 18, nx = j < 12 ? 1 : 2;
        const int xs = nx == 2 ? u >> 1 : 0;
        const int chm = xc * 72 + j * 4 + u, chx0 = xc * 72 + 56 + (j < 12 ? j : 2 * j - 12);
        const int ch = w < 4 ? chm : chx0 + xs, slot = w < 4 ? u : 4 + xs;
        const u32x4 rk = mk_rsrc(ssk + (size_t)chm * S, S * 2), rk0 = mk_rsrc(ssk + (size_t)chx0 * S, S * 2), rk1 = mk_rsrc(ssk + (size_t)(chx0 + nx - 1) * S, S * 2);
        auto conv_k = [&](int c) {
            auto d = [&](u32x4 rs, unsigned dst, unsigned so) { dma16(rs, dst + c * 4160 + 64, vl16 + 4 + 4 * c, so); };
#pragma unroll
            for (int i = 0; i < 4; ++i) d(rk, u * 16640 + i * 1024, i * 1024);
            d(rk0, 4 * 16640 + u * 1024, u * 1024);
            if (nx == 2) d(rk1, 5 * 16640 + u * 1024, u * 1024);
        };
        if (idx < 27) {
            const int tb = xc, c0 = j * 64;
            const int rg = l >> 3, c = l & 7;
            constexpr int TO = 83968;
            u32x4 sk[4];
            LDSP char* kb = lds + w * 9 * 1152 + c * 16;
            {
                const u32x4 rsk = mk_rsrc(shk, D * 8), rki = mk_rsrc(kin, S * D * 2), rvi = mk_rsrc(vin, S * D * 2);
#pragma unroll
                for (int i = 0; i < 4; ++i) ld16(sk[i], rsk, (c0 + c * 8) * 8, i * 16);
                const unsigned vo = (tb * 256 + w * 32 + (l >> 3)) * 1152 + c0 * 2 + (l & 7) * 16;
                dma16n(rki, w * 9 * 1152, vo - 9216, 0);
#pragma unroll
                for (int i = 0; i < 4; ++i) { dma16n(rki, (w * 9 + 1 + i) * 1152, vo, i * 9216); dma16n(rvi, (w * 9 + 5 + i) * 1152, vo, i * 9216); }
            }
            scalars(ch);
            asm volatile("s_waitcnt vmcnt(9)" : "+v"(sk[0]), "+v"(sk[1]), "+v"(sk[2]), "+v"(sk[3]) :: "memory");
            f32x2 sf[4][4];
#pragma unroll
            for (int e = 0; e < 4; ++e) {
                sf[e][0] = f32x2{bflo(sk[e][0]), bflo(sk[e][2])}; sf[e][1] = f32x2{bfhi(sk[e][0]), bfhi(sk[e][2])};
                sf[e][2] = f32x2{bflo(sk[e][1]), bflo(sk[e][3])}; sf[e][3] = f32x2{bfhi(sk[e][1]), bfhi(sk[e][3])};
                asm volatile("" : "+v"(sf[e][0]), "+v"(sf[e][1]), "+v"(sf[e][2]), "+v"(sf[e][3]));
            }
            wait_vm<0>();
            STAMP(1);
            u32x4 kr[7], vr[4];
#pragma unroll
            for (int i = 0; i < 7; ++i) { const int jj = rg + 1 + ((i + 1) >> 2); kr[i] = *(LDSP u32x4*)(kb + (jj >> 1) * 1152 + ((jj & 1) * 4 + ((i + 1) & 3)) * 128); }
#pragma unroll
            for (int i = 0; i < 4; ++i) vr[i] = *(LDSP u32x4*)(kb + (5 + (rg >> 1)) * 1152 + ((rg & 1) * 4 + i) * 128);
            LDSP char* tw = lds + TO + c * (8 * 512 + 16) + (63 - w * 8 - rg) * 8;
#pragma unroll
            for (int e = 0; e < 4; ++e) {
                f32x2 kk[7], uu[4];
#pragma unroll
                for (int i = 0; i < 7; ++i) kk[i] = f32x2{bflo(kr[i][e]), bfhi(kr[i][e])};
#pragma unroll
                for (int i = 0; i < 4; ++i) uu[i] = kk[i] * sf[e][3];
#pragma unroll
                for (int jj = 1; jj < 4; ++jj)
#pragma unroll
                    for (int i = 0; i < 4; ++i) uu[i] = __builtin_elementwise_fma(kk[i + jj], sf[e][3 - jj], uu[i]);
#pragma unroll
                for (int i = 0; i < 4; ++i) uu[i] *= f32x2{bflo(vr[i][e]), bfhi(vr[i][e])};
#pragma unroll
                for (int z = 0; z < 2; ++z) *(LDSP u32x2*)(tw + (2 * e + z) * 512) = u32x2{pk2(uu[3][z], uu[2][z]), pk2(uu[1][z], uu[0][z])};
            }
            STAMP(2);
            lds_bar();
            STAMP(3);
            u32x4 uo[4];
#pragma unroll
            for (int i = 0; i < 4; ++i) { const int cl = i * 16 + (tid >> 5); uo[i] = *(LDSP u32x4*)(lds + TO + cl * 512 + (cl >> 3) * 16 + (tid & 31) * 16); }
#pragma unroll
            for (int i = 0; i < 4; ++i) st16c(uo[i], rws, (tid >> 5) * TP + (tid & 31) * 16, O_U + (c0 + i * 16) * TP + (1792 - tb * 256) * 2);
            STAMP(4);
            wait_bar<0>();
            if (tid == 0) st4c(tag, rws, 0, O_FA + b * 128);
        } else {
            const int n = xc * 5 + idx - 27, row = n * 16 + w * 2 + (l >> 5);
            u32x4 wv[3];
            const u32x4 rw2 = mk_rsrc(w2, D * D * 2);
#pragma unroll
            for (int i = 0; i < 3; ++i) ld16(wv[i], rw2, (i == 2 && (l & 31) >= 8) ? 0x40000000u : row * 1152 + (l & 31) * 16, i * 512);
            scalars(ch);
            asm volatile("s_waitcnt vmcnt(0)" : "+v"(wv[0]), "+v"(wv[1]), "+v"(wv[2]));
            float sm = 0.f;
#pragma unroll
            for (int i = 0; i < 12; ++i) sm += bflo(wv[i >> 2][i & 3]) + bfhi(wv[i >> 2][i & 3]);
#pragma unroll
            for (int m = 1; m < 32; m <<= 1) sm += __shfl_xor(sm, m);
            sm *= 1.f / D;
            const float hi = rbf(sm);
            if (n < 36 && (l & 31) < 2) st2c(pk2((l & 1) ? sm - hi : hi, 0.f), rws, row * 2 + (l & 1) * 1152, O_RHO);
            if (n == 0 && tid < 144) st16c(u32x4{0u, 0u, 0u, 0u}, rws, tid * 16, O_H - 2304);
            wait_bar<0>();
            if (tid == 0) st4c(tag, rws, 0, O_FA + b * 128);
            warm<0>(ssk + (size_t)xc * 72 * S, 72 * S * 2, (idx - 27) * 512 + tid, 2560, 72 * 32, 4096, 32, 0);
        }
        STAMP(5);
        if (w >= 4) { conv_k(0); conv_k(1); conv_k(2); conv_k(3); }
        if (l < 16) {
            const int c = l >> 2;
            u32x4 o = {0u, 0u, 0u, 0u};
            if ((l & 3) == 3) {
                o[3] = c == 0 ? k0[0] : c == 1 ? k0[1] : c == 2 ? k0[2] : k0[3];
                o[2] = c == 0 ? 0u : c == 1 ? k0[0] : c == 2 ? k0[1] : k0[2];
                o[1] = c <= 1 ? 0u : c == 2 ? k0[0] : k0[1];
                o[0] = c <= 2 ? 0u : k0[0];
            }
            *(LDSP u32x4*)(lds + slot * 16640 + c * 4160 + (l & 3) * 16) = o;
        }
        if (l < 56) *(LDSP u32x4*)(lds + CV_U + slot * 9152 + (l / 28) * 4576 + 4096 + (l % 28) * 16) = u32x4{0u, 0u, 0u, 0u};
        const int trips = w < 4 ? 4 : nx, t0 = w < 4 ? 0 : nx == 2 ? (u & 1) * 4 : u * 2;
        LDSP char* ab = opaque(lds + slot * 16640 + (r & 3) * 4160 + ((r >> 2) + g) * 16 + t0 * 64);
        LDSP char* bb = opaque(lds + CV_U + slot * 9152 + (r & 1) * 4576 + (28 - 4 * (r >> 1) + g) * 16 + t0 * 64);
        u32x4 dv;
#pragma unroll
        for (int e = 0; e < 4; ++e) dv[e] = (30 - 2 * r - 8 * g == 2 * e && t0 == 0) ? dD : 0u;
        asm volatile("" : "+v"(dv));
        const bf16x8 da = __builtin_bit_cast(bf16x8, dv);
        auto put_y = [&](const f32x4& a0, const f32x4& a1, int cy, int cp) {
            st8c(u32x2{pk2(a0[0], a0[1]), pk2(a0[2], a0[3])}, rws, l * 8, O_Y + cy * TP + cp * 1024);
            st8c(u32x2{pk2(a1[0], a1[1]), pk2(a1[2], a1[3])}, rws, l * 8, O_Y + cy * TP + cp * 1024 + 512);
        };
        unsigned vt[3], snk = 0;
#pragma unroll
        for (int i = 0; i < 3; ++i) { const int ln = (i * 14 + j) * 256 + tid; vt[i] = (ln / 18) * 9216 + (xc & 3) * 2304 + (ln % 18) * 128; pin(vt[i]); }
        const u32x4 rw3 = mk_rsrc(w3, D * F * 2);
        if (w < 4) {
            await(~tag, tag, rws, O_FA + ((18 + ((l < 8 ? chm : chx0 + xs) >> 6)) * 8 + (l & 7)) * 128);
            STAMP(6);
            u32x4 ur[6]; unsigned nw[6];
            const int e0 = nx == 2 ? (u & 1) * 2048 : u * 1024, e1 = nx == 2 ? e0 + 1024 : e0;
            const u32x4 rux = mk_rsrc(ws + O_U + (chx0 + xs) * TP, 4096);
#pragma unroll
            for (int i = 0; i < 4; ++i) ld16c(ur[i], rws, vl16, O_U + chm * TP + i * 1024);
            ld16c(ur[4], rux, vl16, e0); ld16c(ur[5], rux, vl16, e1);
            ld4c(nw[5], rux, 1024, e1);
            asm volatile("s_waitcnt vmcnt(0)" : "+v"(ur[0]), "+v"(ur[1]), "+v"(ur[2]), "+v"(ur[3]), "+v"(ur[4]), "+v"(ur[5]), "+v"(nw[5]));
            STAMP(8);
#pragma unroll
            for (int i = 0; i < 3; ++i) nw[i] = __builtin_amdgcn_readfirstlane(ur[i + 1][0]);
            nw[3] = 0u; nw[4] = nx == 2 ? __builtin_amdgcn_readfirstlane(ur[5][0]) : nw[5];
#pragma unroll
            for (int i = 0; i < 6; ++i) {
                LDSP char* uw = lds + CV_U + l * 16 + (i < 4 ? u * 9152 + i * 1024 : (4 + xs) * 9152 + (i == 4 ? e0 : e1));
                const unsigned nx1 = __builtin_amdgcn_update_dpp(nw[i], ur[i][0], 0x130, 0xf, 0xf, false);
                if (i == 5 && nx == 1) break;
                *(LDSP u32x4*)(uw + 4576) = ur[i];
                *(LDSP u32x4*)uw = u32x4{__builtin_amdgcn_alignbit(ur[i][1], ur[i][0], 16), __builtin_amdgcn_alignbit(ur[i][2], ur[i][1], 16),
                                         __builtin_amdgcn_alignbit(ur[i][3], ur[i][2], 16), __builtin_amdgcn_alignbit(nx1, ur[i][3], 16)};
            }
        }
        LDSP unsigned* cnt = (LDSP unsigned*)(lds + 163836);
        if (tid == 256) *cnt = 0u;
        if (w >= 4) __builtin_amdgcn_s_setprio(1);
        wait_bar_l<0>();
        STAMP(7);
        f32x4 acc[8];
        {
            bf16x8 A[2][8], B[2][8];
            auto ldf = [&](int t, int e) {
                if (e & 1) A[t & 1][e >> 1] = *(LDSP bf16x8*)(ab + (t + 8 * (e >> 1)) * 64);
                else B[t & 1][e >> 1] = *(LDSP bf16x8*)(bb + (7 - (e >> 1)) * 512 + t * 64);
            };
#pragma unroll
            for (int e = 0; e < 16; ++e) ldf(0, e);
#pragma unroll
            for (int n = 0; n < 8; ++n) mfma1<true>(acc[n], B[0][n], da);
            SB();
#pragma unroll 1
            for (int it = 0; it < trips; ++it) {
                if (w < 4 && it < 3) asm volatile("buffer_load_dword %0, %1, %2, 0 offen" : "+v"(snk) : "v"(vt[0]), "s"(rw3));
                vt[0] = vt[1]; vt[1] = vt[2];
#pragma unroll
                for (int t = 0; t < 2; ++t) {
                    int cnt = 0;
#pragma unroll
                    for (int m = 0; m < 8; ++m)
#pragma unroll
                        for (int n = 0; n + m < 8; ++n) {
                            mfma1<false>(acc[m + n], B[t][n], A[t][m]);
                            if ((cnt & 1) == 0 && cnt < 32) ldf(t + 1, cnt >> 1);
                            ++cnt;
                            SB();
                        }
                }
                ab += 128; bb += 128;
            }
        }
        asm volatile("s_waitcnt lgkmcnt(0)\n\ts_nop 7" : "+v"(acc[0]), "+v"(acc[1]), "+v"(acc[2]), "+v"(acc[3]), "+v"(acc[4]), "+v"(acc[5]), "+v"(acc[6]), "+v"(acc[7]) :: "memory");
        STAMP(9);
        if (w < 4) {
#pragma unroll
            for (int i = 0; i < 4; ++i) put_y(acc[2 * i], acc[2 * i + 1], chm, i);
            asm volatile("s_waitcnt vmcnt(8)" :: "v"(snk));
        } else {
            auto meet = [&](unsigned n) {
                asm volatile("s_waitcnt lgkmcnt(0)" ::: "memory");
                if (l == 0) __hip_atomic_fetch_add(cnt, 1u, __ATOMIC_RELAXED, __HIP_MEMORY_SCOPE_WORKGROUP);
#pragma unroll 1
                for (int it = 0; it < (1 << 20) && __hip_atomic_load(cnt, __ATOMIC_RELAXED, __HIP_MEMORY_SCOPE_WORKGROUP) < n; ++it) __builtin_amdgcn_s_sleep(1);
                asm volatile("" ::: "memory");
            };
            LDSP char* pb = lds + 4 * 16640 + l * 16;
            __builtin_amdgcn_s_setprio(0);
            meet(4);
#pragma unroll
            for (int c = 0; c < 8; ++c) *(LDSP f32x4*)(pb + (u * 8 + c) * 1024) = acc[c];
            meet(8);
            f32x4 pp[2][4];
#pragma unroll
            for (int i = 0; i < 8; ++i) pp[i & 1][i >> 1] = *(LDSP f32x4*)(pb + ((i >> 1) * 8 + u * 2 + (i & 1)) * 1024);
            const f32x4 a0 = pp[0][0] + pp[0][1], a1 = pp[1][0] + pp[1][1], b0 = pp[0][2] + pp[0][3], b1 = pp[1][2] + pp[1][3];
            if (nx == 1) put_y(a0 + b0, a1 + b1, chx0, u);
            else { put_y(a0, a1, chx0, u); put_y(b0, b1, chx0 + 1, u); }
        }
        STAMP(10);
    }
    DUMP(0, 12); }
    const int fc = 2 * (xc & 3) + (idx & 1), rt = (xc >> 2) * 16 + (idx >> 1);
    const int wi = w >> 2, wj = w & 3;
    const int rho = rt % 9;
    DECL; constexpr unsigned smask = 0xa0100a; (void)smask;
    const u32x4 r3 = mk_rsrc((const char*)w3 + fc * 1152 + (size_t)w * 8 * 9216, D * F * 2 - (fc * 1152 + w * 8 * 9216));
    const u32x4 r4 = mk_rsrc((const char*)w4 + (size_t)fc * 576 * 1152, 576 * 1152);
    auto row4 = [&](int kk) { return kk == 8 ? wrap9(8 + rho) * 64 + w * 8 : wrap9((kk & ~1) + ((w >> 1) & 1) + rho) * 64 + ((kk & 1) * 2 + (w >> 2)) * 16 + (w & 1) * 8; };
    const unsigned vsw = ((l & 7) ^ (g << 1)) * 16;
    const unsigned vo3 = (l >> 3) * 9216 + vsw, vo4 = (l >> 3) * 1152 + vsw;
    const unsigned ldsw = w * 1024;
    auto sigma = [&](int pos) { return pos == 0 ? 8 : ((fc + pos) & 7); };
    auto issue = [&](auto N) {
        constexpr int n = N.value, st = n / 9, kk = n % 9;
        if constexpr (st < 9) dma16(r3, ldsw + (st & 1) * HALF + kk * 8192, vo3, kk * 64 * 9216 + wrap9(st + rho) * 128);
        else dma16(r4, ldsw + (st & 1) * HALF + kk * 8192, vo4, row4(kk) * 1152 + wrap9(sigma(st - 9) + rho) * 128);
    };
    unsigned fl, vxf = O_FB + ((((rt >> 1) & 1) * 9 + min(l, 8)) * 8 + (rt >> 2)) * 128;
    pin(vxf);
    lds_bar();
    if (idx >= 18) ld_flag(fl, w, rws, vxf); else fl = ~tag;
    sfor<0, 3>(issue);
    if (idx < 18) {
        wait_bar<3>();
        st_flag(tag, w, rws, O_FB + b * 128);
    }
    STAMP(0);
    sfor<3, 9>(issue);
    const int cs_own = wrap9(fc + rho), cs_sh = wrap9(8 + rho);
    auto yflag = [&](int c) { const int cl = c % 72; return O_FB + ((18 + (cl < 56 ? cl >> 2 : cl < 68 ? cl - 56 : (cl - 44) >> 1)) * 8 + c / 72) * 128; };
    unsigned vyf = yflag(w == 0 ? cs_own * 64 + l : cs_sh * 64 + (fc & 3) * 16 + r);
    unsigned vpf = O_FP + (((rt & 15) * 2 + (l & 1)) * 8 + (rt >> 4) * 4 + ((l >> 1) & 3)) * 128;
    if (w >= 4) vpf = O_FB + (tid - 256) * 128;
    const int pskip = w == 0 ? 0 : w >= 4 ? b : 1;
    unsigned vy = (l >> 3) * TP + (l & 7) * 16, vg = r * 4;
    pin(vyf); pin(vpf); pin(vy); pin(vg);
    LDSP char* gr = lds + wi * 4096 + l * 16;
    LDSP char* gw = opaque(lds + (wj >> 1) * 8192 + wi * 4096 + (wj & 1) * 1024 + l * 16);
    LDSP char* tb = lds + (4 * g + q) * 128 + (wj ^ sw) * 32 + p * 8;
    LDSP char* tbs[3] = {opaque(tb), opaque(tb + 65536), opaque(tb + 131072)};
    LDSP char* grs[3] = {opaque(gr), opaque(gr + 65536), opaque(gr + 131072)};
    chk_flag<9>(fl, w, tag, rws, vxf);
    bar();
    STAMP(1);
    u32x4 sr[3];
    auto issue_x = [&](int i) { dma16c(rws, HALF + i * 8192 + ldsw, vl16, O_X + (rt * 9 + i) * 8192 + ldsw); };
#pragma unroll
    for (int i = 0; i < XPRE; ++i) { if (i == 4) bar(); issue_x(i); }
    STAMP(2);
    bf16x8 X[36];
    auto ldw = [&](auto KS) -> bf16x8 {
        constexpr int st = KS.value / 18, ks = KS.value % 18, off = (st & 1) * HALF + ks * 4096;
        return ld_tr(tbs[off >> 16] + (off & 65535), 2048);
    };
    auto ldx = [&](auto KS) {
        constexpr int gk = KS.value, ks = gk % 18;
        if constexpr (gk < 18 || (gk >= 162 && gk < 180)) {
            constexpr int off = (gk < 18 ? HALF : 0) + (ks >> 1) * 8192 + (ks & 1) * 1024;
            LDSP char* a = grs[off >> 16] + (off & 65535);
            X[2 * ks] = *(LDSP bf16x8*)a; X[2 * ks + 1] = *(LDSP bf16x8*)(a + 2048);
        }
    };
    bf16x8 Wf[3];
    unsigned Gown[9][4];
    f32x4 acc[2][2];
    GeluT T;
    float base[8], rs[2], mu[2], ngs[2];
    auto putg = [&]() {
#pragma unroll
        for (int i = 0; i < 8; ++i) {
            const int s = i & ~1, t = i & 1;
            *(LDSP u32x4*)(gw + s * 8192 + t * 2048) = u32x4{Gown[s][2 * t], Gown[s][2 * t + 1], Gown[s + 1][2 * t], Gown[s + 1][2 * t + 1]};
        }
        SB();
    };
    auto last_g = [&]() {
#pragma unroll
        for (int i = 0; i < 4; ++i) Gown[8][i] = gelu2(acc[0][i >> 1][(i & 1) * 2], acc[0][i >> 1][(i & 1) * 2 + 1], rs[i >> 1]);
        LDSP char* g8 = lds + 65536 + wi * 4096 + (wj >> 1) * 1024 + l * 16 + (wj & 1) * 8;
        *(LDSP u32x2*)g8 = u32x2{Gown[8][0], Gown[8][1]}; *(LDSP u32x2*)(g8 + 2048) = u32x2{Gown[8][2], Gown[8][3]};
        SB();
    };
    const unsigned pt = O_PART + rt * 9 * 8 * 8 * 1024 + w * 1024;
    auto mine = [&](int pos) { return pt + (unsigned)((sigma(pos) * 8 + fc) * 8192); };
    auto theirs = [&](int pos) { return pt + (unsigned)((fc * 8 + ((fc - pos) & 7)) * 8192); };
    auto sx_of = [&](int cs) { return O_X + (unsigned)((rt * 9 + cs) * 8192 + wi * 4096 + (wj >> 1) * 1024); };
    auto sq_of = [&](int cs) { return O_Q + (unsigned)(((rt * 2 + wi) * 36 + cs * 4 + wj) * 1024); };
    constexpr int YO = 2 * HALF, YS = YO + 9728;
    auto y_at = [&](int off, int t, int e) { return (unsigned)*(LDSP u16*)(lds + off + (4 * g + e) * 128 + t * 64 + (r >> 1) * 8 + wi * 4 + (r & 1) * 2); };
    Side sd, se;
    u32x4 pv[8], ev[8], sel[3];
    unsigned gt[2], yv[2][2];

    auto put_part = [&](const f32x4* P, int pos) {
        u32x4 o = {pk2(P[0][0], P[0][1]), pk2(P[0][2], P[0][3]), pk2(P[1][0], P[1][1]), pk2(P[1][2], P[1][3])};
        if (pos >= 3) {
#pragma unroll
            for (int e = 0; e < 4; ++e) o[e] = min(o[e], 0xfffffffeu);
        }
        st16c(o, rws, vl16, mine(pos));
    };
    auto side = [&]() {
        ld16c(sd.q, rws, vl16, sq_of(cs_own)); ld16c(sd.x[0], rws, vl16, sx_of(cs_own)); ld16c(sd.x[1], rws, vl16, sx_of(cs_own) + 2048);
        ld4c(gt[0], rws, vg, O_GATE + (rt * 64 + wi * 32) * 4); ld4c(gt[1], rws, vg, O_GATE + (rt * 64 + wi * 32 + 16) * 4);
        dma16c_if(w >> 1, rws, YS + ldsw, vy, O_Y + (cs_sh * 64 + (fc & 3) * 16 + w * 8) * TP + rt * 128);
        dma16c(rws, YO + ldsw, vy, O_Y + (cs_own * 64 + w * 8) * TP + rt * 128);
    };

    sfor<0, 18>([&](auto ST) {
        constexpr int st = ST.value;
        sfor<0, 9>([&](auto KK) {
            constexpr int kk = KK.value, n = st * 9 + kk, gk = 2 * n;
            if constexpr (is_bar(n)) {
                static_assert(SCH.vm[n] >= 0 && SCH.vm[n] < 64, "schedule");
                if constexpr (n == N_YCHK || n == N_CHK) {
                    chk_flag<(n == N_CHK ? SCH.pollwait : SCH.ywait)>(fl, n == N_CHK ? pskip : w >> 1, tag, rws, n == N_CHK ? vpf : vyf);
                }
                wait_bar<SCH.vm[n]>();
                if constexpr (n == 3) { st_flag(tag, idx < 18 ? 1 : w, rws, O_FB + b * 128); pin(sink); }
                if constexpr (n == N_PUB) st_flag(tag, w, rws, O_FP + b * 128);
                if constexpr (n == N_CHK) st_flag(tag, b | w, rws, 0);
                if constexpr (n == 0) { ldx(IC(0)); Wf[0] = ldw(IC(0)); ldx(IC(1)); Wf[1] = ldw(IC(1)); }
                if constexpr (n == 81) {
                    if (w < 4) { putg(); last_g(); } else { last_g(); putg(); }
                    side();
                    lds_bar();
                    ldx(IC(162)); ldx(IC(163));
                }
            }
            if constexpr (n == N_YPOLL) ld_flag(fl, w >> 1, rws, vyf);
            if constexpr (n == N_POLL) ld_flag(fl, pskip, rws, vpf);
            if constexpr (kk == 0) STAMP(3 + st);
            SB();
            f32x4* A = acc[st & 1];
            f32x4* P = acc[(st & 1) ^ 1];
            const bf16x8 Wa = Wf[gk % 3], Wb = Wf[(gk + 1) % 3];
            mfma1<kk == 0>(A[0], Wa, X[4 * kk]);
            SB();
            if constexpr (n < 161) { Wf[(gk + 2) % 3] = ldw(IC(gk + 2)); if constexpr (n != 80) ldx(IC(gk + 2)); SB(); }
            mfma1<kk == 0>(A[1], Wa, X[4 * kk + 1]);
            SB();
            if constexpr (n < 3 && XPRE + 2 * n < 9) { issue_x(XPRE + 2 * n); SB(); }
            if constexpr (n == 153) { put_part(P, 7); SB(); }
            if constexpr (SCH.d1[n] >= 0) { issue(IC(SCH.d1[n] >= 0 ? SCH.d1[n] : 0)); SB(); }
            mfma1<false>(A[0], Wb, X[4 * kk + 2]);
            SB();
            if constexpr (n < 161) { Wf[gk % 3] = ldw(IC(gk + 3)); if constexpr (n != 80) ldx(IC(gk + 3)); SB(); }
            mfma1<false>(A[1], Wb, X[4 * kk + 3]);
            SB();
            if constexpr (n < 3 && XPRE + 2 * n + 1 < 9) { issue_x(XPRE + 2 * n + 1); SB(); }
            if constexpr (n == 2) {
#pragma unroll
                for (int i = 0; i < 3; ++i) ld16c(sr[i], rws, (i == 2 && g) ? 0x40000000u : vl16, O_STAT + (rt * 2 + wi) * 2304 + i * 1024);
            }
            if constexpr (SCH.d2[n] >= 0) { issue(IC(SCH.d2[n] >= 0 ? SCH.d2[n] : 0)); SB(); }
            if constexpr (st >= 1 && st <= 8) gelu_blk<kk>(T, P, rs, Gown[st - 1]);
            if constexpr (n == 8) {
                u32x4* s3 = sr;
                asm volatile("" : "+v"(s3[0]), "+v"(s3[1]), "+v"(s3[2]));
#pragma unroll
                for (int t = 0; t < 2; ++t) {
                    const float s2 = sum_g(__uint_as_float(sr[0][t]) + __uint_as_float(sr[1][t]) + __uint_as_float(sr[2][t]));
                    rs[t] = inv_std(s2 * (GINV * GINV / D));
                    mu[t] = __uint_as_float(sr[0][2 + t]);
                }
                float* r2 = rs;
                asm volatile("" : "+v"(r2[0]), "+v"(r2[1]));
            }
            if constexpr (n_store(n) && n != 153) put_part(P, st - 10);
            if constexpr (fill_at(n) >= 0) st16c(u32x4{~0u, ~0u, ~0u, ~0u}, rws, vl16, mine(fill_at(n)));
            if constexpr (n >= 100 && n < 104) { yv[n & 1][0] = y_at(YO + wj * 2048, (n - 100) >> 1, (n & 1) * 2); yv[n & 1][1] = y_at(YO + wj * 2048, (n - 100) >> 1, (n & 1) * 2 + 1); }
            if constexpr (n == 100) {
                pin(gt[0]); pin(gt[1]);
#pragma unroll
                for (int t = 0; t < 2; ++t) { ngs[t] = rbf(1.f - __uint_as_float(gt[t])) * GINV; mu[t] *= ngs[t]; }
            }
            if constexpr (n >= 101 && n < 105) {
                constexpr int e = n - 101;
                mix_base(base[2 * e], base[2 * e + 1], sd.q[e], yv[e & 1][0], yv[e & 1][1], __uint_as_float(gt[e >> 1]), mu[e >> 1]);
            }
            sfor<1, 8>([&](auto I) { if constexpr (get_n(I.value) == n) ld16c(pv[I.value], rws, vl16, theirs(I.value)); });
            if constexpr (n == NSH) { ld_shared(ev, se, w - fc, rws, vl16, pt + 64 * 8192, sx_of(cs_sh), sq_of(cs_sh)); }
            if constexpr (n == 154) {
                const unsigned one = (r >> 2) == g ? ((r & 1) ? 0x3f800000u : 0x3f80u) : 0u;
                const unsigned o0 = (r & 2) ? 0u : one, o1 = (r & 2) ? one : 0u, m = (wj & 1) ? ~0u : 0u;
                sel[0] = u32x4{o0, o1, 0u, 0u};
                sel[1] = u32x4{0u, 0u, o0, o1};
                sel[2] = u32x4{o0 & ~m, o1 & ~m, o0 & m, o1 & m};
                u32x4* ss = sel;
                asm volatile("" : "+v"(ss[0]), "+v"(ss[1]), "+v"(ss[2]));
            }
            if constexpr (n == 155) add2(A[0], A[1], sel[2], sel[2], sd.x[0], sd.x[1]);
            if constexpr (n == N_ADD) {
                u32x4* pp = pv;
                asm volatile("s_waitcnt vmcnt(%3)" : "+v"(pp[3]), "+v"(pp[4]), "+v"(pp[5]) : "n"(SCH.endwait));
                if (__builtin_expect(__builtin_amdgcn_ballot_w64(max(max(umax4(pv[3]), umax4(pv[4])), umax4(pv[5])) == 0xffffffffu) != 0, 0)) {
#pragma unroll
                    for (int i = 3; i < 6; ++i) settle(pv[i], theirs(i), rws, vl16);
                }
            }
            sfor<1, 6>([&](auto I) { if constexpr (n == (N_ADD + I.value < 161 ? N_ADD + I.value : 161)) add2(A[0], A[1], sel[0], sel[1], pv[I.value], pv[I.value]); });
            SB();
        });
    });
    STAMP(21);
    const u32x4 ro = mk_rsrc(out + (size_t)rt * 64 * D, 64 * D * 2);
    auto mix = [&](const float* bs, const f32x4* sum, LDSP char* dst, int pitch) {
#pragma unroll
        for (int t = 0; t < 2; ++t)
            *(LDSP u32x2*)(dst + (t * 16 + r) * pitch + g * 8) =
                u32x2{pk2(bs[4 * t] + ngs[t] * sum[t][0], bs[4 * t + 1] + ngs[t] * sum[t][1]), pk2(bs[4 * t + 2] + ngs[t] * sum[t][2], bs[4 * t + 3] + ngs[t] * sum[t][3])};
    };
    u32x4 ve;
    if (w == fc) {
        f32x4 c[2] = {};
        float be[8];
        asm volatile("s_waitcnt vmcnt(1)\n\ts_nop 1" : "+v"(c[0]), "+v"(c[1]));
#pragma unroll
        for (int i = 0; i < 8; ++i) add2(c[0], c[1], sel[0], sel[1], ev[i], ev[i]);
        add2(c[0], c[1], sel[2], sel[2], se.x[0], se.x[1]);
#pragma unroll
        for (int e = 0; e < 4; ++e) mix_base(be[2 * e], be[2 * e + 1], se.q[e], y_at(YS, e >> 1, (e & 1) * 2), y_at(YS, e >> 1, (e & 1) * 2 + 1), __uint_as_float(gt[e >> 1]), mu[e >> 1]);
        asm volatile("" : "+v"(c[0]), "+v"(c[1]));
        LDSP char* scr = lds + 2 * HALF + 12288;
        mix(be, c, scr, 48);
        ve = *(LDSP u32x4*)(scr + (l >> 1) * 48 + (l & 1) * 16);
    }
    {
        asm volatile("s_waitcnt vmcnt(0)" : "+v"(pv[6]), "+v"(pv[7]));
        settle(pv[6], theirs(6), rws, vl16);
        settle(pv[7], theirs(7), rws, vl16);
        STAMP(22);
        if (w == fc) st16c(ve, ro, (wi * 32 + (l >> 1)) * 1152 + wj * 32 + (l & 1) * 16, cs_sh * 128);
        asm volatile("s_nop 1" : "+v"(pv[6]), "+v"(pv[7]));
#pragma unroll
        for (int i = 6; i < 8; ++i) add2(acc[1][0], acc[1][1], sel[0], sel[1], pv[i], pv[i]);
        asm volatile("s_nop 7\n\ts_nop 1" : "+v"(acc[1][0]), "+v"(acc[1][1]));
        LDSP char* scr = lds + 2 * HALF;
        mix(base, acc[1], scr + wi * 32 * 144 + wj * 32, 144);
        lds_bar();
        st16c(*(LDSP u32x4*)(scr + (tid >> 3) * 144 + (l & 7) * 16), ro, (tid >> 3) * 1152 + (l & 7) * 16, cs_own * 128);
    }
    asm volatile("k_all_code_hi:");
    DRAIN(); STAMP(23); DUMP(16, 24);
}

extern "C" void kernel_launch(void* const* d_in, const int* in_sizes, int n_in, void* d_out, int out_size, void* d_ws, size_t ws_size, hipStream_t stream) {
    (void)hipFuncSetAttribute((const void*)k_all, hipFuncAttributeMaxDynamicSharedMemorySize, 163840);
    k_all<<<256, 512, 163840, stream>>>((const u16*)d_in[0], (const u16*)d_in[1], (const u16*)d_in[2], (const u16*)d_in[3], (const u16*)d_in[4], (const u16*)d_in[5],
                                        (const u16*)d_in[7], (const u16*)d_in[8], (const u16*)d_in[9], (const u16*)d_in[10], (const u16*)d_in[11],
                                        (char*)d_ws, (u16*)d_out);
}
```
